```python
import torch

def _softmax(x, axis=-1):
    return torch.softmax(x, dim=axis)

def _gelu(x):
    return torch.nn.functional.gelu(x, approximate='tanh')

def _silu(x):
    return torch.nn.functional.silu(x)

def _sigmoid(x):
    return torch.sigmoid(x)

def _layernorm(x, axis=-1, eps=1e-05):
    m = x.mean(dim=axis, keepdim=True)
    v = x.var(dim=axis, keepdim=True, unbiased=False)
    return (x - m) / torch.sqrt(v + eps)

def _scatter_add(tgt, idx, src, axis=0):
    return tgt.clone().index_add_(axis, idx, src)

def _segment_sum(src, seg, n):
    out = torch.zeros((n,) + src.shape[1:], dtype=src.dtype)
    return out.index_add_(0, seg, src)

def _pad(x, pw):
    flat = tuple((v for pair in reversed(pw) for v in pair))
    return torch.nn.functional.pad(x, flat)
kernel_name = 'adaLN_cond__sliding_local_dilated__mixer_channel_mlp__prompt'

def setup_inputs():

    _w = lambda a: (lambda t: t.to(torch.bfloat16) if t.is_floating_point() else t)(torch.as_tensor(a))

    def _A_setup():
        B, S, D = (1, 2048, 640)
        return {'x': _w((lambda *s: torch.randn(*s, dtype=torch.bfloat16) * 0.05)(B, S, D)), 'wqkv': _w((lambda *s: torch.randn(*s, dtype=torch.bfloat16) * 0.05)(D, 3 * D)), 'wo': _w((lambda *s: torch.randn(*s, dtype=torch.bfloat16) * 0.05)(D, D))}
    _A_in = _A_setup()

    def _B_setup():
        B, S, D, F = (1, 2048, 640, 1728)
        return {'x': _w((lambda *s: torch.randn(*s, dtype=torch.bfloat16) * 0.05)(B, S, D)), 'w1': _w((lambda *s: torch.randn(*s, dtype=torch.bfloat16) * 3.2)(D, F) * D ** (-0.5)), 'b1': _w((lambda *s: torch.randn(*s, dtype=torch.bfloat16) * 1.6)(F)), 'w2': _w((lambda *s: torch.randn(*s, dtype=torch.bfloat16) * 0.4)(F, D) * F ** (-0.5)), 'b2': _w((lambda *s: torch.randn(*s, dtype=torch.bfloat16) * 0.8)(D))}
    _B_in = _B_setup()
    return {'A_x': _A_in['x'], 'A_wqkv': _A_in['wqkv'], 'A_wo': _A_in['wo'], 'B_x': _B_in['x'], 'B_w1': _B_in['w1'], 'B_b1': _B_in['b1'], 'B_w2': _B_in['w2'], 'B_b2': _B_in['b2'], 'c': _w((lambda *s: torch.randn(*s, dtype=torch.bfloat16) * 0.1)(1, 640) * 8.0), 'w_mod': _w((lambda *s: torch.randn(*s, dtype=torch.bfloat16) * 0.1)(640, 6 * 640) * 640 ** (-0.5) * 8.0)}

def reference(A_x, A_wqkv, A_wo, B_x, B_w1, B_b1, B_w2, B_b2, c, w_mod):

    def _A_ref(x, wqkv, wo):
        B, S, D = x.shape
        H = 32
        Dh = D // H
        W, dil = (8, 2)
        qkv = (lambda x, *d: x.permute(*d))((x @ wqkv).reshape(B, S, 3, H, Dh), 2, 0, 3, 1, 4)
        q, k, v = (qkv[0], qkv[1], qkv[2])
        scale = Dh ** (-0.5)
        pos = torch.arange(S)
        off = torch.arange(W) * dil
        idx = pos[:, None] - off[None, :]
        mask = idx >= 0
        idx_c = torch.where(mask, idx, 0).reshape(-1)
        kg = k[:, :, idx_c, :].reshape(B, H, S, W, Dh)
        vg = v[:, :, idx_c, :].reshape(B, H, S, W, Dh)
        logits = torch.einsum('bhsd,bhswd->bhsw', q, kg) * scale
        logits = torch.where(mask[None, None, :, :], logits, -1000000000.0)
        attn = _softmax(logits, axis=-1)
        o = torch.einsum('bhsw,bhswd->bhsd', attn, vg)
        return (lambda x, *d: x.permute(*d))(o, 0, 2, 1, 3).reshape(B, S, D) @ wo

    def _B_ref(x, w1, b1, w2, b2):
        y = _layernorm(x, axis=-1)
        h = _gelu(torch.einsum('bsd,df->bsf', y, w1) + b1)
        return torch.einsum('bsf,fd->bsd', h, w2) + b2
    x = A_x
    mod = (_silu(c) @ w_mod).reshape(c.shape[0], 6, 1, -1)
    sa, ca, ga, sb, cb, gb = (mod[:, 0].clone(), mod[:, 1].clone(), mod[:, 2].clone(), mod[:, 3].clone(), mod[:, 4].clone(), mod[:, 5].clone())
    h = _layernorm(x, axis=-1) * (1 + ca) + sa
    x = x + ga * _A_ref(h, A_wqkv, A_wo)
    h = _layernorm(x, axis=-1) * (1 + cb) + sb
    x = x + gb * _B_ref(h, B_w1, B_b1, B_w2, B_b2)
    return x
```

```cpp
#include <hip/hip_runtime.h>
#include <hip/hip_bf16.h>
#include <stdint.h>

typedef unsigned short u16;
constexpr int S = 2048, D = 640, F = 1728, H = 32, DH = 20;

__device__ __forceinline__ float bf2f(u16 v) { return __uint_as_float(((unsigned)v) << 16); }
__device__ __forceinline__ u16 f2bf(float f) { return __builtin_bit_cast(u16, (__bf16)f); }

__global__ void k_mod(const u16* c, const u16* wm, float* mod) {
  int j = blockIdx.x * 256 + threadIdx.x;
  float acc = 0.f;
  for (int k = 0; k < D; ++k) {
    float cv = bf2f(c[k]);
    float s = cv / (1.f + __expf(-cv));
    acc += bf2f(f2bf(s)) * bf2f(wm[(size_t)k * 6 * D + j]);
  }
  mod[j] = acc;
}

__device__ float block_sum(float v, float* sm) {
  for (int o = 32; o > 0; o >>= 1) v += __shfl_xor(v, o);
  __syncthreads();
  if ((threadIdx.x & 63) == 0) sm[threadIdx.x >> 6] = v;
  __syncthreads();
  float r = 0.f;
  for (int i = 0; i < (int)(blockDim.x >> 6); ++i) r += sm[i];
  return r;
}

__global__ void k_ln(const float* xin, const u16* xbf, const float* sh, const float* sc, u16* out, int second) {
  __shared__ float sm[16];
  int row = blockIdx.x, t = threadIdx.x;
  float x = xin ? xin[(size_t)row * D + t] : bf2f(xbf[(size_t)row * D + t]);
  float m = block_sum(x, sm) * (1.f / D);
  float d = x - m;
  float v = block_sum(d * d, sm) * (1.f / D);
  float h = d * rsqrtf(v + 1e-5f) * (1.f + sc[t]) + sh[t];
  if (second) {
    float m2 = block_sum(h, sm) * (1.f / D);
    float d2 = h - m2;
    float v2 = block_sum(d2 * d2, sm) * (1.f / D);
    h = d2 * rsqrtf(v2 + 1e-5f);
  }
  out[(size_t)row * D + t] = f2bf(h);
}

__global__ void k_gemm(const u16* A, const u16* B, float* C, int M, int N, int K) {
  __shared__ float sa[16][17], sb[16][17];
  int tx = threadIdx.x & 15, ty = threadIdx.x >> 4;
  int row = blockIdx.y * 16 + ty, col = blockIdx.x * 16 + tx;
  float acc = 0.f;
  for (int k0 = 0; k0 < K; k0 += 16) {
    sa[ty][tx] = bf2f(A[(size_t)row * K + k0 + tx]);
    sb[ty][tx] = bf2f(B[(size_t)(k0 + ty) * N + col]);
    __syncthreads();
    for (int k = 0; k < 16; ++k) acc += sa[ty][k] * sb[k][tx];
    __syncthreads();
  }
  C[(size_t)row * N + col] = acc;
}

__global__ void k_attn(const float* qkv, u16* o) {
  int idx = blockIdx.x * 256 + threadIdx.x;
  int s = idx / H, h = idx % H;
  const float* q = qkv + (size_t)s * 3 * D + h * DH;
  float lg[8];
  float mx = -1e30f;
  for (int w = 0; w < 8; ++w) {
    int p = s - 2 * w;
    float a = 0.f;
    if (p >= 0) {
      const float* k = qkv + (size_t)p * 3 * D + D + h * DH;
      for (int d = 0; d < DH; ++d) a += q[d] * k[d];
      a *= 0.22360679774997896f;
    } else a = -1e9f;
    lg[w] = a; mx = fmaxf(mx, a);
  }
  float sum = 0.f;
  for (int w = 0; w < 8; ++w) { lg[w] = __expf(lg[w] - mx); sum += lg[w]; }
  float inv = 1.f / sum;
  for (int d = 0; d < DH; ++d) {
    float a = 0.f;
    for (int w = 0; w < 8; ++w) {
      int p = s - 2 * w; if (p < 0) p = 0;
      a += lg[w] * qkv[(size_t)p * 3 * D + 2 * D + h * DH + d];
    }
    o[(size_t)s * D + h * DH + d] = f2bf(a * inv);
  }
}

__global__ void k_res1(const u16* x, const float* A, const float* ga, float* x1) {
  int i = blockIdx.x * 256 + threadIdx.x;
  x1[i] = bf2f(x[i]) + ga[i % D] * A[i];
}
__global__ void k_gelu(const float* h, const u16* b1, u16* out) {
  int i = blockIdx.x * 256 + threadIdx.x;
  float v = h[i] + bf2f(b1[i % F]);
  float u = 0.7978845608028654f * (v + 0.044715f * v * v * v);
  float e = __expf(2.f * u);
  float th = 1.f - 2.f / (e + 1.f);
  out[i] = f2bf(0.5f * v * (1.f + th));
}
__global__ void k_res2(const float* x1, const float* B, const u16* b2, const float* gb, u16* out) {
  int i = blockIdx.x * 256 + threadIdx.x;
  out[i] = f2bf(x1[i] + gb[i % D] * (B[i] + bf2f(b2[i % D])));
}

extern "C" void kernel_launch(void* const* d_in, const int* in_sizes, int n_in,
                              void* d_out, int out_size, void* d_ws, size_t ws_size,
                              hipStream_t stream) {
  const u16* x = (const u16*)d_in[0];
  const u16* wqkv = (const u16*)d_in[1];
  const u16* wo = (const u16*)d_in[2];
  const u16* w1 = (const u16*)d_in[4];
  const u16* b1 = (const u16*)d_in[5];
  const u16* w2 = (const u16*)d_in[6];
  const u16* b2 = (const u16*)d_in[7];
  const u16* c = (const u16*)d_in[8];
  const u16* wm = (const u16*)d_in[9];
  char* ws = (char*)d_ws;
  float* mod = (float*)ws;
  u16* h = (u16*)(ws + (1 << 16));
  float* qkv = (float*)(ws + (4 << 20));
  u16* o = (u16*)(ws + (24 << 20));
  float* A = (float*)(ws + (28 << 20));
  float* x1 = (float*)(ws + (36 << 20));
  u16* y = (u16*)(ws + (44 << 20));
  float* hh = (float*)(ws + (48 << 20));
  u16* hb = (u16*)(ws + (64 << 20));
  float* B = (float*)(ws + (72 << 20));
  k_mod<<<6 * D / 256, 256, 0, stream>>>(c, wm, mod);
  k_ln<<<S, D, 0, stream>>>(nullptr, x, mod + 0 * D, mod + 1 * D, h, 0);
  k_gemm<<<dim3(3 * D / 16, S / 16), 256, 0, stream>>>(h, wqkv, qkv, S, 3 * D, D);
  k_attn<<<S * H / 256, 256, 0, stream>>>(qkv, o);
  k_gemm<<<dim3(D / 16, S / 16), 256, 0, stream>>>(o, wo, A, S, D, D);
  k_res1<<<S * D / 256, 256, 0, stream>>>(x, A, mod + 2 * D, x1);
  k_ln<<<S, D, 0, stream>>>(x1, nullptr, mod + 3 * D, mod + 4 * D, y, 1);
  k_gemm<<<dim3(F / 16, S / 16), 256, 0, stream>>>(y, w1, hh, S, F, D);
  k_gelu<<<S * F / 256, 256, 0, stream>>>(hh, b1, hb);
  k_gemm<<<dim3(D / 16, S / 16), 256, 0, stream>>>(hb, w2, B, S, D, F);
  k_res2<<<S * D / 256, 256, 0, stream>>>(x1, B, b2, mod + 5 * D, (u16*)d_out);
}
```

```cpp
#include <hip/hip_runtime.h>

typedef unsigned short u16;
typedef unsigned int u32;
typedef unsigned long long u64;
typedef u32 v4u __attribute__((ext_vector_type(4)));
typedef u32 v2u __attribute__((ext_vector_type(2)));
typedef float f32x4 __attribute__((ext_vector_type(4)));
typedef float f32x2 __attribute__((ext_vector_type(2)));
typedef short s16x8 __attribute__((ext_vector_type(8)));
typedef short s16x4 __attribute__((ext_vector_type(4)));
typedef __bf16 bf16x2 __attribute__((ext_vector_type(2)));
#define GAS __attribute__((address_space(1)))
#define LAS __attribute__((address_space(3)))
#define DI __device__ __forceinline__
typedef __amdgpu_buffer_rsrc_t rsrc_t;

#define XN 2

constexpr int NWG = 256, NTHR = 512;
constexpr u32 OFF_E = 0, OFF_F0 = 0x1000, OFF_F1 = 0xA000, OFF_F2 = 0x12000, OFF_F3 = 0x1A000;
constexpr u32 OFF_MODP = 0x30000, OFF_WQ = 0x40000, OFF_WO = 0x300000, OFF_W1 = 0x400000, OFF_W2 = 0x700000;
constexpr u32 OFF_O = 0xA00000, OFF_X1 = 0xD00000, OFF_HH = 0x1000000, WS_USED = 0x1800000;
constexpr u32 OFF_DIAG = 0x1800000;
constexpr int L_A = 0, L_VEC = 102400, L_OB = 112640, L_RED = 123904, L_GV = 144384, L_SM = 153600, LDS_BYTES = 163840;
constexpr int QS = 488;
constexpr int OS = 176;
constexpr int SPIN_CAP = 1 << 14;
constexpr int SC1 = 16;

DI f32x4 mfma(v4u a, v4u b, f32x4 c) {
  return __builtin_amdgcn_mfma_f32_16x16x32_bf16(__builtin_bit_cast(s16x8, a), __builtin_bit_cast(s16x8, b), c, 0, 0, 0);
}
DI u32 pack2(float a, float b) { f32x2 v = {a, b}; return __builtin_bit_cast(u32, __builtin_convertvector(v, bf16x2)); }
DI float blo(u32 u) { return __builtin_bit_cast(float, u << 16); }
DI float bhi(u32 u) { return __builtin_bit_cast(float, u & 0xffff0000u); }
DI f32x2 unpk(u32 u) { return f32x2{blo(u), bhi(u)}; }
DI f32x2 fma2(f32x2 a, f32x2 b, f32x2 c) { return __builtin_elementwise_fma(a, b, c); }
DI u32 pk(f32x2 v) { return __builtin_bit_cast(u32, __builtin_convertvector(v, bf16x2)); }
DI float dot2(u32 a, u32 b, float c) { return __builtin_amdgcn_fdot2_f32_bf16(__builtin_bit_cast(bf16x2, a), __builtin_bit_cast(bf16x2, b), c, false); }
DI float bf2f(u16 v) { return __builtin_bit_cast(float, (u32)v << 16); }
template <int CTRL> DI float dpp_add(float v) {
  return v + __builtin_bit_cast(float, __builtin_amdgcn_update_dpp(0, __builtin_bit_cast(int, v), CTRL, 0xf, 0xf, true));
}
DI float row16_sum(float v) { v = dpp_add<0x128>(v); v = dpp_add<0x124>(v); v = dpp_add<0x122>(v); return dpp_add<0x121>(v); }
DI float x16_sum(float v) {
  auto r = __builtin_amdgcn_permlane16_swap(__builtin_bit_cast(u32, v), __builtin_bit_cast(u32, v), false, false);
  const u32 a = r[0], c = r[1];
  return __builtin_bit_cast(float, a) + __builtin_bit_cast(float, c);
}
DI float x32_sum(float v) {
  auto r = __builtin_amdgcn_permlane32_swap(__builtin_bit_cast(u32, v), __builtin_bit_cast(u32, v), false, false);
  const u32 a = r[0], c = r[1];
  return __builtin_bit_cast(float, a) + __builtin_bit_cast(float, c);
}
DI float x16_max(float v) {
  auto r = __builtin_amdgcn_permlane16_swap(__builtin_bit_cast(u32, v), __builtin_bit_cast(u32, v), false, false);
  const u32 a = r[0], c = r[1];
  return fmaxf(__builtin_bit_cast(float, a), __builtin_bit_cast(float, c));
}
DI float x32_max(float v) {
  auto r = __builtin_amdgcn_permlane32_swap(__builtin_bit_cast(u32, v), __builtin_bit_cast(u32, v), false, false);
  const u32 a = r[0], c = r[1];
  return fmaxf(__builtin_bit_cast(float, a), __builtin_bit_cast(float, c));
}
DI void bar() { asm volatile("s_waitcnt lgkmcnt(0)\n\ts_barrier" ::: "memory"); }
DI void drain() { asm volatile("s_waitcnt vmcnt(0)" ::: "memory"); }
#define SCHED() __builtin_amdgcn_sched_barrier(0)
DI v4u ldw(rsrc_t rs, u32 voff, u32 soff) { return __builtin_amdgcn_raw_buffer_load_b128(rs, voff, soff, SC1); }
DI void stw(rsrc_t rs, v4u v, u32 voff, u32 soff) { __builtin_amdgcn_raw_buffer_store_b128(v, rs, voff + soff, 0, SC1); }
DI v4u lds128(LAS unsigned char* L, int off) { return *(LAS v4u*)(L + off); }
DI f32x4 ldsf4(LAS unsigned char* L, int off) { return *(LAS f32x4*)(L + off); }
DI v2u lds64(LAS unsigned char* L, int off) { return *(LAS v2u*)(L + off); }
DI v2u ldstr(LAS unsigned char* L, int off) {
  return __builtin_bit_cast(v2u, __builtin_amdgcn_ds_read_tr16_b64_v4i16((LAS s16x4*)(L + off)));
}
template <int N> DI void dma(rsrc_t rs, u32 voff, u32 soff, u32 ldsoff) {
  if constexpr (N == 4)
    asm volatile("s_mov_b32 m0, %0\n\ts_nop 0\n\tbuffer_load_dwordx4 %1, %2, %3 offen sc1 lds\n\tbuffer_load_dwordx4 %1, %2, %3 offen offset:1024 sc1 lds\n\t"
                 "buffer_load_dwordx4 %1, %2, %3 offen offset:2048 sc1 lds\n\tbuffer_load_dwordx4 %1, %2, %3 offen offset:3072 sc1 lds"
                 :: "s"(ldsoff), "v"(voff), "s"(rs), "s"(soff) : "memory");
  else
    asm volatile("s_mov_b32 m0, %0\n\ts_nop 0\n\tbuffer_load_dwordx4 %1, %2, %3 offen sc1 lds\n\tbuffer_load_dwordx4 %1, %2, %3 offen offset:1024 sc1 lds\n\t"
                 "buffer_load_dwordx4 %1, %2, %3 offen offset:2048 sc1 lds"
                 :: "s"(ldsoff), "v"(voff), "s"(rs), "s"(soff) : "memory");
}
DI void poll(unsigned char* ws, u32 off, int n, int lane, u64 T) {
  const GAS u64* f = (const GAS u64*)(ws + off + (lane < n ? lane : 0) * 128);
#pragma unroll 1
  for (int spins = 0; spins < SPIN_CAP; ++spins) {
    const u64 v = __hip_atomic_load(f, __ATOMIC_RELAXED, __HIP_MEMORY_SCOPE_AGENT);
    if (__all(v == T)) break;
    __builtin_amdgcn_s_sleep(1);
  }
  __builtin_amdgcn_fence(__ATOMIC_ACQUIRE, "wavefront");
}
DI void publish(unsigned char* ws, u32 off, u64 T) {
  __hip_atomic_store((GAS u64*)(ws + off), T, __ATOMIC_RELAXED, __HIP_MEMORY_SCOPE_AGENT);
}
DI void row_stats(const v4u (&r)[5], float& rstd, float& nb) {
  float s0 = 0.f, s1 = 0.f, q0 = 0.f, q1 = 0.f;
#pragma unroll
  for (int m = 0; m < 5; ++m) {
    s0 = dot2(r[m].x, 0x3f803f80u, s0); q0 = dot2(r[m].x, r[m].x, q0);
    s1 = dot2(r[m].y, 0x3f803f80u, s1); q1 = dot2(r[m].y, r[m].y, q1);
    s0 = dot2(r[m].z, 0x3f803f80u, s0); q0 = dot2(r[m].z, r[m].z, q0);
    s1 = dot2(r[m].w, 0x3f803f80u, s1); q1 = dot2(r[m].w, r[m].w, q1);
  }
  const float mean = row16_sum(s0 + s1) * (1.f / 640.f);
  const float var = fmaxf(row16_sum(q0 + q1) * (1.f / 640.f) - mean * mean, 0.f);
  rstd = __builtin_amdgcn_rsqf(var + 1e-5f);
  nb = -mean * rstd;
}

#define STAMP(i) asm volatile("; MARK " #i ::: "memory")

__global__ __launch_bounds__(NTHR, 2) void fused_block(const u16* __restrict__ x, const u16* __restrict__ wqkv, const u16* __restrict__ wo,
                                                       const u16* __restrict__ w1, const u16* __restrict__ b1, const u16* __restrict__ w2,
                                                       const u16* __restrict__ b2, const u16* __restrict__ cvec, const u16* __restrict__ wmod,
                                                       u16* __restrict__ out, unsigned char* ws) {
  extern __shared__ __attribute__((aligned(16))) unsigned char smem[];
  LAS unsigned char* L = (LAS unsigned char*)smem;
  const int tid = threadIdx.x, lane = tid & 63, b = blockIdx.x;
  const int w = __builtin_amdgcn_readfirstlane(tid >> 6);
  const int xcd = b & 7, jj = b >> 3;
  const int p = (xcd >> 1) * 8 + (jj >> 2), g = (xcd & 1) * 4 + (jj & 3);
  const rsrc_t rs = __builtin_amdgcn_make_buffer_rsrc(ws, 0, WS_USED, 0x00020000);
  const int g4 = lane >> 4, t16 = lane & 15;
  const u32 l16 = lane * 16;
  STAMP(0);

  const u64 Tv = __hip_atomic_load((const GAS u64*)(ws + OFF_E), __ATOMIC_RELAXED, __HIP_MEMORY_SCOPE_AGENT) + 1;
  const int job = w * 256 + b;
  const bool hasjob = job < 1612;
  const GAS unsigned char* jsrc; u32 jstride, jdst, jA, jB; int ppr; bool paired;
  if (job < 480) {
    const int gg = job / 60, rem = job % 60, part = rem / 20, j = rem % 20;
    jsrc = (const GAS unsigned char*)wqkv + (size_t)(32 * j) * 3840 + part * 1280 + gg * 160;
    jstride = 3840; ppr = 10; paired = false;
    jdst = OFF_WQ + (gg * 15 + part * 5) * 20480 + j * 1024; jA = 40960; jB = 20480;
  } else if (job < 640) {
    const int id = job - 480, gg = id / 20, j = id % 20;
    jsrc = (const GAS unsigned char*)wo + (size_t)(32 * j) * 1280 + gg * 160;
    jstride = 1280; ppr = 10; paired = true;
    jdst = OFF_WO + (gg * 20 + j) * 5120; jA = 2048; jB = 1024;
  } else if (job < 1072) {
    const int id = job - 640, gg = id / 54, j = id % 54;
    jsrc = (const GAS unsigned char*)w2 + (size_t)(32 * j) * 1280 + gg * 160;
    jstride = 1280; ppr = 10; paired = true;
    jdst = OFF_W2 + (gg * 54 + j) * 5120; jA = 2048; jB = 1024;
  } else {
    const int id = hasjob ? job - 1072 : 0, jb2 = id / 20, j = id % 20;
    jsrc = (const GAS unsigned char*)w1 + (size_t)(32 * j) * 3456 + jb2 * 128;
    jstride = 3456; ppr = 8; paired = true;
    jdst = OFF_W1 + (2 * jb2 * 20 + j) * 2048; jA = 40960; jB = 1024;
  }
  v4u jr[5];
  if (hasjob) {
    const u32 magic = ppr == 10 ? 6554u : 8192u;
#pragma unroll
    for (int it = 0; it < 5; ++it) {
      u32 idx = it * 64 + lane;
      if (it == 4 && ppr == 8) idx = lane;
      const u32 row = (idx * magic) >> 16, pc = idx - row * ppr;
      jr[it] = *(const GAS v4u*)(jsrc + row * jstride + pc * 16);
    }
  }
  v4u gw[3]; float gs[3];
  const int cl = b % 60, ks = b / 60;
  if (b < 240) {
#pragma unroll
    for (int i = 0; i < 3; ++i) {
      const int rr = (lane >> 3) + 8 * i;
      const int k = 160 * ks + 20 * w + (rr < 20 ? rr : 0);
      gw[i] = *(const GAS v4u*)((const GAS unsigned char*)wmod + (size_t)k * 7680 + cl * 128 + (lane & 7) * 16);
      const float c = bf2f(*(const GAS u16*)((const GAS unsigned char*)cvec + k * 2));
      gs[i] = rr < 20 ? c : 0.f;
    }
  }
  v4u xr[3][5];
#pragma unroll
  for (int a = 0; a < 3; ++a) {
    if (a < 2 || w < 4) {
      int tok = 64 * p - 16 + 4 * (w + 8 * a) + g4;
      tok = tok < 0 ? 0 : tok;
#pragma unroll
      for (int m = 0; m < 5; ++m) xr[a][m] = *(const GAS v4u*)((const GAS unsigned char*)x + (u32)(tok * 1280 + (t16 + 16 * m) * 16));
    } else {
#pragma unroll
      for (int m = 0; m < 5; ++m) xr[a][m] = v4u{0, 0, 0, 0};
    }
  }
  u32 pf = 0, pf2 = 0;
  if (w == 6) pf2 = *(const volatile GAS u32*)((const GAS unsigned char*)out + (u32)((64 * p + lane) * 1280 + g * 160));
  if (w == 7) {
    u64 pc; int len;
    asm volatile("fused_pf_begin:\n\ts_getpc_b64 %0\n\ts_mov_b32 %1, fused_pf_end-fused_pf_begin" : "=s"(pc), "=s"(len));
    const int o = (jj * 16 + t16) * 128;
    if (o < len) pf = *(const GAS u32*)(pc + (u32)o);
  }
  SCHED();
  if (b < 240) {
    float acc[8];
#pragma unroll
    for (int e = 0; e < 8; ++e) acc[e] = 0.f;
#pragma unroll
    for (int i = 0; i < 3; ++i) {
      const float c = gs[i];
      const float s = c * __builtin_amdgcn_rcpf(1.f + __expf(-c));
#pragma unroll
      for (int e = 0; e < 4; ++e) { acc[2 * e] += s * blo(gw[i][e]); acc[2 * e + 1] += s * bhi(gw[i][e]); }
    }
#pragma unroll
    for (int e = 0; e < 8; ++e) acc[e] = x32_sum(x16_sum(dpp_add<0x128>(acc[e])));
    if (lane < 8) {
      *(LAS f32x4*)(L + L_GV + w * 256 + lane * 32) = f32x4{acc[0], acc[1], acc[2], acc[3]};
      *(LAS f32x4*)(L + L_GV + w * 256 + lane * 32 + 16) = f32x4{acc[4], acc[5], acc[6], acc[7]};
    }
  }
  if (hasjob) {
    LAS unsigned char* sc = L + w * 5120;
#pragma unroll
    for (int it = 0; it < 5; ++it)
      if (it < 4 || ppr == 10) *(LAS v4u*)(sc + it * 1024 + l16) = jr[it];
    __builtin_amdgcn_fence(__ATOMIC_RELEASE, "wavefront");
    __builtin_amdgcn_wave_barrier();
    __builtin_amdgcn_fence(__ATOMIC_ACQUIRE, "wavefront");
    const int rowb = ppr * 16;
    const int q = t16 >> 2, pp = lane & 3;
    const int rbase = (8 * g4 + q) * rowb;
#pragma unroll
    for (int ct = 0; ct < 5; ++ct) {
      if (ct < 4 || ppr == 10) {
        const int cb = (paired && ct < 4) ? 64 * (ct >> 1) + 8 * (ct & 1) + 16 * pp : 32 * ct + 8 * pp;
        const v2u lo = ldstr(sc, rbase + cb), hi = ldstr(sc, rbase + 4 * rowb + cb);
        stw(rs, v4u{lo.x, lo.y, hi.x, hi.y}, l16, jdst + (ct >> 1) * jA + (ct & 1) * jB);
      }
    }
  }
  bar();
  if (b < 240 && w == 0) {
    float s = 0.f;
#pragma unroll
    for (int ww = 0; ww < 8; ++ww) s += *(LAS float*)(L + L_GV + ww * 256 + lane * 4);
    __builtin_amdgcn_raw_buffer_store_b32(__builtin_bit_cast(u32, s), rs, lane * 4, OFF_MODP + (ks * 3840 + cl * 64) * 4, SC1);
  }
  drain();
  asm volatile("" ::"v"(pf), "v"(pf2));
  bar();
  const u32 Tlo = __builtin_amdgcn_readfirstlane((u32)Tv), Thi = __builtin_amdgcn_readfirstlane((u32)(Tv >> 32));
  const u64 T = ((u64)Thi << 32) | Tlo;
  if (tid == 0) publish(ws, OFF_F0 + b * 128, T);
  STAMP(1);
#pragma unroll
  for (int a = 0; a < 3; ++a) {
    if (a < 2 || w < 4) {
      float rstd, nb;
      row_stats(xr[a], rstd, nb);
      const f32x2 r2 = {rstd, rstd}, n2 = {nb, nb};
#pragma unroll
      for (int m = 0; m < 5; ++m)
#pragma unroll
        for (int e = 0; e < 4; ++e) xr[a][m][e] = pk(fma2(unpk(xr[a][m][e]), r2, n2));
    }
  }
  STAMP(2);
  if (w < 4) poll(ws, OFF_F0 + w * 64 * 128, 64, lane, T);
  bar();
  STAMP(3);
  if (b == 0 && tid == 0) publish(ws, OFF_E, T);

  if (tid < 320) {
    f32x4 s = {0.f, 0.f, 0.f, 0.f};
    v4u pv[4];
#pragma unroll
    for (int k = 0; k < 4; ++k) pv[k] = ldw(rs, tid * 16, OFF_MODP + k * 15360);
#pragma unroll
    for (int k = 0; k < 4; ++k) s += __builtin_bit_cast(f32x4, pv[k]);
    if (tid >= 160) s += 1.f;
    *(LAS f32x4*)(L + L_VEC + tid * 16) = s;
  }
  constexpr int R1 = 8;
  v4u wr[R1][2];
  const u32 wq0 = OFF_WQ + (g * 15 + 2 * w) * 20480, wq1 = OFF_WQ + (g * 15 + (w < 7 ? 2 * w + 1 : 14)) * 20480;
#pragma unroll
  for (int j = 0; j < R1; ++j) { wr[j][0] = ldw(rs, l16 + (j & 3) * 1024, wq0 + (j >> 2) * 4096); wr[j][1] = ldw(rs, l16 + (j & 3) * 1024, wq1 + (j >> 2) * 4096); }
  SCHED();
  bar();
  f32x4 acc1[5][2];
#pragma unroll
  for (int i = 0; i < 5; ++i) { acc1[i][0] = f32x4{0.f, 0.f, 0.f, 0.f}; acc1[i][1] = f32x4{0.f, 0.f, 0.f, 0.f}; }
  const int rd_even = g4 * 256 + ((t16 ^ g4) * 16), rd_odd = rd_even ^ 64;
  auto prep1 = [&](int m) {
    const int k0 = (t16 + 16 * m) * 8;
    const f32x4 s0 = ldsf4(L, L_VEC + k0 * 4), s1 = ldsf4(L, L_VEC + k0 * 4 + 16);
    const f32x4 c0 = ldsf4(L, L_VEC + 2560 + k0 * 4), c1 = ldsf4(L, L_VEC + 2560 + k0 * 4 + 16);
#pragma unroll
    for (int a = 0; a < 3; ++a) {
      if (a < 2 || w < 4) {
        const int grp = w + 8 * a, tt = grp >> 2, t = 4 * (grp & 3) + g4;
        const v4u r = xr[a][m];
        v4u o;
        o.x = pk(fma2(unpk(r.x), c0.xy, s0.xy));
        o.y = pk(fma2(unpk(r.y), c0.zw, s0.zw));
        o.z = pk(fma2(unpk(r.z), c1.xy, s1.xy));
        o.w = pk(fma2(unpk(r.w), c1.zw, s1.zw));
        *(LAS v4u*)(L + L_A + tt * 20480 + (4 * m + (t16 >> 2)) * 1024 + (t16 & 3) * 256 + ((t ^ (t16 & 7)) * 16)) = o;
      }
    }
  };
  prep1(0);
  bar();
  STAMP(4);
#pragma unroll
  for (int m = 0; m < 5; ++m) {
    if (m < 4) prep1(m + 1);
#pragma unroll
    for (int q = 0; q < 4; ++q) {
      const int j = 4 * m + q;
      const v4u wa = wr[j % R1][0], wb = wr[j % R1][1];
      if (j + R1 < 20) {
        wr[j % R1][0] = ldw(rs, l16 + ((j + R1) & 3) * 1024, wq0 + ((j + R1) >> 2) * 4096);
        wr[j % R1][1] = ldw(rs, l16 + ((j + R1) & 3) * 1024, wq1 + ((j + R1) >> 2) * 4096);
      }
#pragma unroll
      for (int tt = 0; tt < 5; ++tt) {
        const v4u a = lds128(L, L_A + tt * 20480 + j * 1024 + ((j & 1) ? rd_odd : rd_even));
        acc1[tt][0] = mfma(wa, a, acc1[tt][0]);
        acc1[tt][1] = mfma(wb, a, acc1[tt][1]);
      }
      SCHED();
    }
    bar();
  }
  STAMP(5);
#pragma unroll
  for (int tt = 0; tt < 5; ++tt)
#pragma unroll
    for (int c = 0; c < 2; ++c)
      if (c == 0 || w < 7)
        *(LAS v2u*)(L + L_A + (tt * 16 + t16) * QS + (2 * w + c) * 32 + g4 * 8) =
            v2u{pack2(acc1[tt][c].x, acc1[tt][c].y), pack2(acc1[tt][c].z, acc1[tt][c].w)};
  bar();
  STAMP(6);
  {
    const int h = w & 3, par = w >> 2;
    const float cs = 0.22360679774997896f * 1.4426950408889634f;
#pragma unroll
    for (int qt = 0; qt < 2; ++qt) {
      const int qrow = 2 * (8 + 16 * qt + t16) + par;
      const int qo = L_A + qrow * QS + 40 * h + (g4 < 3 ? 16 * g4 : 0);
      v2u qlo = lds64(L, qo), qhi = lds64(L, qo + 8);
      if (g4 == 3) qlo = v2u{0, 0};
      if (g4 >= 2) qhi = v2u{0, 0};
      const v4u qf = {qlo.x, qlo.y, qhi.x, qhi.y};
      f32x4 sc[2];
#pragma unroll
      for (int kt = 0; kt < 2; ++kt) {
        int ki = 16 * qt + 16 * kt + t16;
        ki = ki > 39 ? 39 : ki;
        const int ko = L_A + (2 * ki + par) * QS + 160 + 40 * h + 16 * g4;
        const v2u klo = lds64(L, ko), khi = lds64(L, ko + 8);
        sc[kt] = mfma(v4u{klo.x, klo.y, khi.x, khi.y}, qf, f32x4{0.f, 0.f, 0.f, 0.f});
      }
      float mx = -3.0e38f;
#pragma unroll
      for (int kt = 0; kt < 2; ++kt)
#pragma unroll
        for (int r = 0; r < 4; ++r) {
          const int kr = 16 * kt + 4 * g4 + r;
          bool ok = (unsigned)(kr - t16 - 1) < 8u;
          if (qt == 0) ok = ok && (p > 0 || kr >= 8);
          sc[kt][r] = ok ? sc[kt][r] : -3.0e38f;
          mx = fmaxf(mx, sc[kt][r]);
        }
      mx = x32_max(x16_max(mx));
      float sum = 0.f;
#pragma unroll
      for (int kt = 0; kt < 2; ++kt)
#pragma unroll
        for (int r = 0; r < 4; ++r) { sc[kt][r] = __builtin_amdgcn_exp2f((sc[kt][r] - mx) * cs); sum += sc[kt][r]; }
      sum = x32_sum(x16_sum(sum));
      const float inv = __builtin_amdgcn_rcpf(sum);
      const v4u pf4 = {pack2(sc[0].x * inv, sc[0].y * inv), pack2(sc[0].z * inv, sc[0].w * inv), pack2(sc[1].x * inv, sc[1].y * inv), pack2(sc[1].z * inv, sc[1].w * inv)};
      const int vq = t16 >> 2, vp = lane & 3;
      int ka = 16 * qt + 4 * g4 + vq, kb = ka + 16;
      kb = kb > 39 ? 39 : kb;
      const int va = L_A + (2 * ka + par) * QS + 320 + 40 * h, vb = L_A + (2 * kb + par) * QS + 320 + 40 * h;
      const v2u a0 = ldstr(L, va + 8 * vp), b0 = ldstr(L, vb + 8 * vp);
      const v2u a1 = ldstr(L, va + 32), b1v = ldstr(L, vb + 32);
      const f32x4 o0 = mfma(v4u{a0.x, a0.y, b0.x, b0.y}, pf4, f32x4{0.f, 0.f, 0.f, 0.f});
      const f32x4 o1 = mfma(v4u{a1.x, a1.y, b1v.x, b1v.y}, pf4, f32x4{0.f, 0.f, 0.f, 0.f});
      const int orow = L_OB + (par * 32 + 16 * qt + t16) * OS + 40 * h;
      *(LAS v2u*)(L + orow + 8 * g4) = v2u{pack2(o0.x, o0.y), pack2(o0.z, o0.w)};
      if (g4 == 0) *(LAS v2u*)(L + orow + 32) = v2u{pack2(o1.x, o1.y), pack2(o1.z, o1.w)};
    }
  }
  bar();
  {
    const int tok = 2 * (lane & 31) + (lane >> 5);
#pragma unroll
    for (int i = 0; i < 2; ++i) {
      const int u = w + 8 * i;
      if (u < 10) {
        const int ch = 10 * g + u;
        const v4u v = lds128(L, L_OB + lane * OS + 16 * u);
        stw(rs, v, (tok >> 4) * 20480 + (tok & 15) * 16, OFF_O + p * 81920 + (ch >> 2) * 1024 + (ch & 3) * 256);
      }
    }
  }
  drain();
  bar();
  if (tid == 0) publish(ws, OFF_F1 + (p * 8 + g) * 128, T);
  STAMP(7);

  const int s4 = w & 3, kh = w >> 2;
#pragma unroll
  for (int i = 0; i < 4; ++i) {
    const int gi = w + 8 * i;
    if (gi < 25) dma<4>(rs, l16, OFF_WO + g * 102400 + gi * 4096, L_A + gi * 4096);
  }
  const int tokg = 64 * p + 16 * s4 + t16;
  const int fo = kh == 0 ? 8 * g4 : 32 + 8 * g4;
  const v4u xa = *(const GAS v4u*)((const GAS unsigned char*)x + (u32)(tokg * 1280 + (80 * g + fo) * 2));
  const v2u xb = *(const GAS v2u*)((const GAS unsigned char*)x + (u32)(tokg * 1280 + (80 * g + 64 + 4 * g4) * 2));
  if (tid < 320) {
    f32x4 s = {0.f, 0.f, 0.f, 0.f};
    v4u pv[4];
#pragma unroll
    for (int k = 0; k < 4; ++k) pv[k] = ldw(rs, tid * 16, OFF_MODP + k * 15360 + 1920 * 4);
#pragma unroll
    for (int k = 0; k < 4; ++k) s += __builtin_bit_cast(f32x4, pv[k]);
    if (tid >= 160) s += 1.f;
    *(LAS f32x4*)(L + L_VEC + 5120 + tid * 16) = s;
  } else if (tid < 360) {
    const int i = tid - 320, which = i / 20, c4 = i % 20;
    f32x4 s = {0.f, 0.f, 0.f, 0.f};
    v4u pv[4];
#pragma unroll
    for (int k = 0; k < 4; ++k) pv[k] = ldw(rs, (which ? 3200 : 1280) * 4 + c4 * 16, OFF_MODP + k * 15360 + g * 320);
#pragma unroll
    for (int k = 0; k < 4; ++k) s += __builtin_bit_cast(f32x4, pv[k]);
    *(LAS f32x4*)(L + L_SM + which * 320 + c4 * 16) = s;
  } else if (tid >= 384 && tid < 404) {
    const int c4 = tid - 384;
    const v2u v = *(const GAS v2u*)((const GAS unsigned char*)b2 + (80 * g + 4 * c4) * 2);
    *(LAS f32x4*)(L + L_SM + 640 + c4 * 16) = f32x4{blo(v.x), bhi(v.x), blo(v.y), bhi(v.y)};
  }
  if (w == 7) poll(ws, OFF_F1 + p * 8 * 128, 8, lane, T);
  bar();
  STAMP(8);
  v4u oa[10];
#pragma unroll
  for (int i = 0; i < 10; ++i) oa[i] = ldw(rs, l16 + (i & 3) * 1024, OFF_O + (p * 4 + s4) * 20480 + kh * 10240 + (i >> 2) * 4096);
  asm volatile("s_waitcnt vmcnt(10)" ::: "memory");
  bar();
  f32x4 acc2[5];
#pragma unroll
  for (int i = 0; i < 5; ++i) acc2[i] = f32x4{0.f, 0.f, 0.f, 0.f};
#pragma unroll
  for (int i = 0; i < 10; ++i) {
#pragma unroll
    for (int ct = 0; ct < 5; ++ct) acc2[ct] = mfma(lds128(L, L_A + ((10 * kh + i) * 5 + ct) * 1024 + l16), oa[i], acc2[ct]);
  }
  if (kh == 0) {
    *(LAS f32x4*)(L + L_RED + (s4 * 5 + 2) * 1024 + l16) = acc2[2];
    *(LAS f32x4*)(L + L_RED + (s4 * 5 + 3) * 1024 + l16) = acc2[3];
  } else {
    *(LAS f32x4*)(L + L_RED + (s4 * 5 + 0) * 1024 + l16) = acc2[0];
    *(LAS f32x4*)(L + L_RED + (s4 * 5 + 1) * 1024 + l16) = acc2[1];
    *(LAS f32x4*)(L + L_RED + (s4 * 5 + 4) * 1024 + l16) = acc2[4];
  }
  bar();
  STAMP(9);
  v4u x1a; v2u x1b = {0, 0};
  {
    const int c0 = kh == 0 ? 0 : 2;
    const f32x4 u0 = (kh == 0 ? acc2[0] : acc2[2]) + ldsf4(L, L_RED + (s4 * 5 + c0) * 1024 + l16);
    const f32x4 u1 = (kh == 0 ? acc2[1] : acc2[3]) + ldsf4(L, L_RED + (s4 * 5 + c0 + 1) * 1024 + l16);
    const f32x4 g0 = ldsf4(L, L_SM + fo * 4), g1 = ldsf4(L, L_SM + fo * 4 + 16);
    x1a.x = pack2(blo(xa.x) + g0.x * u0.x, bhi(xa.x) + g0.y * u0.y);
    x1a.y = pack2(blo(xa.y) + g0.z * u0.z, bhi(xa.y) + g0.w * u0.w);
    x1a.z = pack2(blo(xa.z) + g1.x * u1.x, bhi(xa.z) + g1.y * u1.y);
    x1a.w = pack2(blo(xa.w) + g1.z * u1.z, bhi(xa.w) + g1.w * u1.w);
    stw(rs, x1a, tokg * 1280 + (80 * g + fo) * 2, OFF_X1);
    if (kh == 0) {
      const f32x4 u2 = acc2[4] + ldsf4(L, L_RED + (s4 * 5 + 4) * 1024 + l16);
      const f32x4 g2 = ldsf4(L, L_SM + (64 + 4 * g4) * 4);
      x1b.x = pack2(blo(xb.x) + g2.x * u2.x, bhi(xb.x) + g2.y * u2.y);
      x1b.y = pack2(blo(xb.y) + g2.z * u2.z, bhi(xb.y) + g2.w * u2.w);
      __builtin_amdgcn_raw_buffer_store_b64(x1b, rs, tokg * 1280 + (80 * g + 64 + 4 * g4) * 2, OFF_X1, SC1);
    }
  }
  drain();
  bar();
  if (tid == 0) publish(ws, OFF_F2 + (p * 8 + g) * 128, T);
  STAMP(10);

  const int j0 = g < 7 ? 7 * g : 48, nk = g < 6 ? 7 : 6;
  const bool act3 = w < nk;
  const int jb = j0 + (act3 ? w : 0);
  constexpr int R3 = 8;
  v4u w3[R3][2];
  const u32 w1o = OFF_W1 + jb * 40960;
#pragma unroll
  for (int j = 0; j < R3; ++j) { w3[j][0] = ldw(rs, l16 + (j & 1) * 2048, w1o + (j >> 1) * 4096); w3[j][1] = ldw(rs, l16 + (j & 1) * 2048 + 1024, w1o + (j >> 1) * 4096); }
  const v4u b1v = *(const GAS v4u*)((const GAS unsigned char*)b1 + (32 * jb + 8 * g4) * 2);
  SCHED();
  if (w == 7) poll(ws, OFF_F2 + p * 8 * 128, 8, lane, T);
  bar();
  STAMP(11);
  v4u yr[2][5];
#pragma unroll
  for (int a = 0; a < 2; ++a)
#pragma unroll
    for (int m = 0; m < 5; ++m) yr[a][m] = ldw(rs, (64 * p + 8 * w + 4 * a + g4) * 1280 + (t16 + 16 * m) * 16, OFF_X1);
  SCHED();
#pragma unroll
  for (int a = 0; a < 2; ++a) {
    asm volatile("" ::: "memory");
    SCHED();
    float rstd, nb;
    row_stats(yr[a], rstd, nb);
    const f32x2 r2 = {rstd, rstd}, n2 = {nb, nb};
#pragma unroll
    for (int m = 0; m < 5; ++m) {
      const int k0 = (t16 + 16 * m) * 8;
      const f32x4 s0 = ldsf4(L, L_VEC + 5120 + k0 * 4), s1 = ldsf4(L, L_VEC + 5120 + k0 * 4 + 16);
      const f32x4 c0 = ldsf4(L, L_VEC + 7680 + k0 * 4), c1 = ldsf4(L, L_VEC + 7680 + k0 * 4 + 16);
      yr[a][m].x = pk(fma2(fma2(unpk(yr[a][m].x), r2, n2), c0.xy, s0.xy));
      yr[a][m].y = pk(fma2(fma2(unpk(yr[a][m].y), r2, n2), c0.zw, s0.zw));
      yr[a][m].z = pk(fma2(fma2(unpk(yr[a][m].z), r2, n2), c1.xy, s1.xy));
      yr[a][m].w = pk(fma2(fma2(unpk(yr[a][m].w), r2, n2), c1.zw, s1.zw));
    }
    row_stats(yr[a], rstd, nb);
    const f32x2 r3 = {rstd, rstd}, n3 = {nb, nb};
    const int row = 8 * w + 4 * a + g4, tt = row >> 4, t = row & 15;
#pragma unroll
    for (int m = 0; m < 5; ++m) {
      const v4u o = {pk(fma2(unpk(yr[a][m].x), r3, n3)), pk(fma2(unpk(yr[a][m].y), r3, n3)),
                     pk(fma2(unpk(yr[a][m].z), r3, n3)), pk(fma2(unpk(yr[a][m].w), r3, n3))};
      *(LAS v4u*)(L + L_A + tt * 20480 + (4 * m + (t16 >> 2)) * 1024 + (t16 & 3) * 256 + ((t ^ (t16 & 7)) * 16)) = o;
    }
  }
  bar();
  STAMP(12);
  f32x4 acc3[4][2];
#pragma unroll
  for (int i = 0; i < 4; ++i) { acc3[i][0] = f32x4{0.f, 0.f, 0.f, 0.f}; acc3[i][1] = f32x4{0.f, 0.f, 0.f, 0.f}; }
  if (act3) {
#pragma unroll
    for (int j = 0; j < 20; ++j) {
      const v4u wa = w3[j % R3][0], wb = w3[j % R3][1];
      if (j + R3 < 20) {
        w3[j % R3][0] = ldw(rs, l16 + ((j + R3) & 1) * 2048, w1o + ((j + R3) >> 1) * 4096);
        w3[j % R3][1] = ldw(rs, l16 + ((j + R3) & 1) * 2048 + 1024, w1o + ((j + R3) >> 1) * 4096);
      }
#pragma unroll
      for (int tt = 0; tt < 4; ++tt) {
        const v4u a = lds128(L, L_A + tt * 20480 + j * 1024 + ((j & 1) ? rd_odd : rd_even));
        acc3[tt][0] = mfma(wa, a, acc3[tt][0]);
        acc3[tt][1] = mfma(wb, a, acc3[tt][1]);
      }
      SCHED();
    }
  }
  bar();
  STAMP(13);
  if (act3) {
    const float bb[8] = {blo(b1v.x), bhi(b1v.x), blo(b1v.y), bhi(b1v.y), blo(b1v.z), bhi(b1v.z), blo(b1v.w), bhi(b1v.w)};
    const float k1 = -2.f * 0.7978845608028654f * 1.4426950408889634f, k3 = k1 * 0.044715f;
#pragma unroll
    for (int tt = 0; tt < 4; ++tt) {
      float hv[8];
#pragma unroll
      for (int e = 0; e < 8; ++e) {
        const float v = acc3[tt][e >> 2][e & 3] + bb[e];
        const float ex = __builtin_amdgcn_exp2f(v * (k1 + k3 * v * v));
        hv[e] = v * __builtin_amdgcn_rcpf(1.f + ex);
      }
      stw(rs, v4u{pack2(hv[0], hv[1]), pack2(hv[2], hv[3]), pack2(hv[4], hv[5]), pack2(hv[6], hv[7])}, l16, OFF_HH + ((p * 4 + tt) * 54 + jb) * 1024);
    }
  }
  drain();
  bar();
  if (tid == 0) publish(ws, OFF_F3 + (p * 8 + g) * 128, T);
  STAMP(14);

  const u32 w2o = OFF_W2 + (g * 54 + 27 * kh) * 5120;
  const u32 ring = kh * 76800;
#pragma unroll
  for (int i = 0; i < 5; ++i) {
    const int gi = s4 + 4 * i;
    if (gi < 18) dma<4>(rs, l16, w2o + gi * 4096, ring + gi * 4096);
    else if (gi == 18) dma<3>(rs, l16, w2o + gi * 4096, ring + gi * 4096);
  }
  if (w == 7) poll(ws, OFF_F3 + p * 8 * 128, 8, lane, T);
  bar();
  STAMP(15);
  v4u ha[27];
#pragma unroll
  for (int i = 0; i < 27; ++i) ha[i] = ldw(rs, l16 + (i & 3) * 1024, OFF_HH + ((p * 4 + s4) * 54 + 27 * kh) * 1024 + (i >> 2) * 4096);
  asm volatile("s_waitcnt vmcnt(27)" ::: "memory");
  bar();
  f32x4 acc4[5];
#pragma unroll
  for (int i = 0; i < 5; ++i) acc4[i] = f32x4{0.f, 0.f, 0.f, 0.f};
#pragma unroll
  for (int n = 0; n < 9; ++n) {
#pragma unroll
    for (int q = 0; q < 3; ++q) {
      const int i = 3 * n + q;
#pragma unroll
      for (int ct = 0; ct < 5; ++ct) acc4[ct] = mfma(lds128(L, ring + ((i % 15) * 5 + ct) * 1024 + l16), ha[i], acc4[ct]);
    }
    if (n == 4) asm volatile("s_waitcnt vmcnt(9)" ::: "memory");
    if (n == 5) asm volatile("s_waitcnt vmcnt(6)" ::: "memory");
    if (n == 6) asm volatile("s_waitcnt vmcnt(3)" ::: "memory");
    if (n == 7) asm volatile("s_waitcnt vmcnt(0)" ::: "memory");
    bar();
    if (n < 4) {
      if (s4 < 3) dma<4>(rs, l16, w2o + (75 + 15 * n + 4 * s4) * 1024, ring + (15 * n + 4 * s4) * 1024);
      else dma<3>(rs, l16, w2o + (75 + 15 * n + 12) * 1024, ring + (15 * n + 12) * 1024);
    }
  }
  STAMP(16);
  if (kh == 0) {
    *(LAS f32x4*)(L + L_RED + (s4 * 5 + 2) * 1024 + l16) = acc4[2];
    *(LAS f32x4*)(L + L_RED + (s4 * 5 + 3) * 1024 + l16) = acc4[3];
  } else {
    *(LAS f32x4*)(L + L_RED + (s4 * 5 + 0) * 1024 + l16) = acc4[0];
    *(LAS f32x4*)(L + L_RED + (s4 * 5 + 1) * 1024 + l16) = acc4[1];
    *(LAS f32x4*)(L + L_RED + (s4 * 5 + 4) * 1024 + l16) = acc4[4];
  }
  bar();
  {
    const int c0 = kh == 0 ? 0 : 2;
    const f32x4 u0 = (kh == 0 ? acc4[0] : acc4[2]) + ldsf4(L, L_RED + (s4 * 5 + c0) * 1024 + l16);
    const f32x4 u1 = (kh == 0 ? acc4[1] : acc4[3]) + ldsf4(L, L_RED + (s4 * 5 + c0 + 1) * 1024 + l16);
    const f32x4 g0 = ldsf4(L, L_SM + 320 + fo * 4), g1 = ldsf4(L, L_SM + 320 + fo * 4 + 16);
    const f32x4 e0 = ldsf4(L, L_SM + 640 + fo * 4), e1 = ldsf4(L, L_SM + 640 + fo * 4 + 16);
    v4u o;
    o.x = pack2(blo(x1a.x) + g0.x * (u0.x + e0.x), bhi(x1a.x) + g0.y * (u0.y + e0.y));
    o.y = pack2(blo(x1a.y) + g0.z * (u0.z + e0.z), bhi(x1a.y) + g0.w * (u0.w + e0.w));
    o.z = pack2(blo(x1a.z) + g1.x * (u1.x + e1.x), bhi(x1a.z) + g1.y * (u1.y + e1.y));
    o.w = pack2(blo(x1a.w) + g1.z * (u1.z + e1.z), bhi(x1a.w) + g1.w * (u1.w + e1.w));
    *(GAS v4u*)((GAS unsigned char*)out + (u32)(tokg * 1280 + (80 * g + fo) * 2)) = o;
    if (kh == 0) {
      const f32x4 u2 = acc4[4] + ldsf4(L, L_RED + (s4 * 5 + 4) * 1024 + l16);
      const f32x4 g2 = ldsf4(L, L_SM + 320 + (64 + 4 * g4) * 4), e2 = ldsf4(L, L_SM + 640 + (64 + 4 * g4) * 4);
      const v2u o2 = {pack2(blo(x1b.x) + g2.x * (u2.x + e2.x), bhi(x1b.x) + g2.y * (u2.y + e2.y)),
                      pack2(blo(x1b.y) + g2.z * (u2.z + e2.z), bhi(x1b.y) + g2.w * (u2.w + e2.w))};
      *(GAS v2u*)((GAS unsigned char*)out + (u32)(tokg * 1280 + (80 * g + 64 + 4 * g4) * 2)) = o2;
    }
  }
  STAMP(17);
  asm volatile("fused_pf_end:" ::: "memory");
}

extern "C" void kernel_launch(void* const* d_in, const int* in_sizes, int n_in, void* d_out, int out_size, void* d_ws, size_t ws_size,
                              hipStream_t stream) {
  (void)in_sizes; (void)n_in; (void)out_size; (void)ws_size;
  (void)hipFuncSetAttribute((const void*)fused_block, hipFuncAttributeMaxDynamicSharedMemorySize, LDS_BYTES);
  fused_block<<<NWG, NTHR, LDS_BYTES, stream>>>((const u16*)d_in[0], (const u16*)d_in[1], (const u16*)d_in[2], (const u16*)d_in[4],
                                                (const u16*)d_in[5], (const u16*)d_in[6], (const u16*)d_in[7], (const u16*)d_in[8],
                                                (const u16*)d_in[9], (u16*)d_out, (unsigned char*)d_ws);
}
```

```cpp
#include <hip/hip_runtime.h>

typedef unsigned short u16;
typedef unsigned int u32;
typedef unsigned long long u64;
typedef u32 v4u __attribute__((ext_vector_type(4)));
typedef u32 v2u __attribute__((ext_vector_type(2)));
typedef float f32x4 __attribute__((ext_vector_type(4)));
typedef float f32x2 __attribute__((ext_vector_type(2)));
typedef short s16x8 __attribute__((ext_vector_type(8)));
typedef short s16x4 __attribute__((ext_vector_type(4)));
typedef __bf16 bf16x2 __attribute__((ext_vector_type(2)));
#define GAS __attribute__((address_space(1)))
#define LAS __attribute__((address_space(3)))
#define DI __device__ __forceinline__
typedef __amdgpu_buffer_rsrc_t rsrc_t;

#define XN 2

constexpr int NWG = 256, NTHR = 512;
constexpr u32 OFF_E = 0, OFF_F0 = 0x1000, OFF_F1 = 0xA000, OFF_F2 = 0x12000, OFF_F3 = 0x1A000;
constexpr u32 OFF_MODP = 0x30000, OFF_WQ = 0x40000, OFF_WO = 0x300000, OFF_W1 = 0x400000, OFF_W2 = 0x700000;
constexpr u32 OFF_O = 0xA00000, OFF_X1 = 0xD00000, OFF_HH = 0x1000000, WS_USED = 0x1800000;
constexpr u32 OFF_DIAG = 0x1800000;
constexpr int L_A = 0, L_VEC = 102400, L_OB = 112640, L_RED = 123904, L_GV = 144384, L_SM = 153600, LDS_BYTES = 163840;
constexpr int QS = 488;
constexpr int OS = 176;
constexpr int SPIN_CAP = 1 << 14;
constexpr int SC1 = 16;

DI f32x4 mfma(v4u a, v4u b, f32x4 c) {
  return __builtin_amdgcn_mfma_f32_16x16x32_bf16(__builtin_bit_cast(s16x8, a), __builtin_bit_cast(s16x8, b), c, 0, 0, 0);
}
DI u32 pack2(float a, float b) { f32x2 v = {a, b}; return __builtin_bit_cast(u32, __builtin_convertvector(v, bf16x2)); }
DI float blo(u32 u) { return __builtin_bit_cast(float, u << 16); }
DI float bhi(u32 u) { return __builtin_bit_cast(float, u & 0xffff0000u); }
DI f32x2 unpk(u32 u) { return f32x2{blo(u), bhi(u)}; }
DI f32x2 fma2(f32x2 a, f32x2 b, f32x2 c) { return __builtin_elementwise_fma(a, b, c); }
DI u32 pk(f32x2 v) { return __builtin_bit_cast(u32, __builtin_convertvector(v, bf16x2)); }
DI float dot2(u32 a, u32 b, float c) { return __builtin_amdgcn_fdot2_f32_bf16(__builtin_bit_cast(bf16x2, a), __builtin_bit_cast(bf16x2, b), c, false); }
DI float bf2f(u16 v) { return __builtin_bit_cast(float, (u32)v << 16); }
template <int CTRL> DI float dpp_add(float v) {
  return v + __builtin_bit_cast(float, __builtin_amdgcn_update_dpp(0, __builtin_bit_cast(int, v), CTRL, 0xf, 0xf, true));
}
DI float row16_sum(float v) { v = dpp_add<0x128>(v); v = dpp_add<0x124>(v); v = dpp_add<0x122>(v); return dpp_add<0x121>(v); }
DI float x16_sum(float v) {
  auto r = __builtin_amdgcn_permlane16_swap(__builtin_bit_cast(u32, v), __builtin_bit_cast(u32, v), false, false);
  const u32 a = r[0], c = r[1];
  return __builtin_bit_cast(float, a) + __builtin_bit_cast(float, c);
}
DI float x32_sum(float v) {
  auto r = __builtin_amdgcn_permlane32_swap(__builtin_bit_cast(u32, v), __builtin_bit_cast(u32, v), false, false);
  const u32 a = r[0], c = r[1];
  return __builtin_bit_cast(float, a) + __builtin_bit_cast(float, c);
}
DI float x16_max(float v) {
  auto r = __builtin_amdgcn_permlane16_swap(__builtin_bit_cast(u32, v), __builtin_bit_cast(u32, v), false, false);
  const u32 a = r[0], c = r[1];
  return fmaxf(__builtin_bit_cast(float, a), __builtin_bit_cast(float, c));
}
DI float x32_max(float v) {
  auto r = __builtin_amdgcn_permlane32_swap(__builtin_bit_cast(u32, v), __builtin_bit_cast(u32, v), false, false);
  const u32 a = r[0], c = r[1];
  return fmaxf(__builtin_bit_cast(float, a), __builtin_bit_cast(float, c));
}
DI void bar() { asm volatile("s_waitcnt lgkmcnt(0)\n\ts_barrier" ::: "memory"); }
DI void drain() { asm volatile("s_waitcnt vmcnt(0)" ::: "memory"); }
#define SCHED() __builtin_amdgcn_sched_barrier(0)
DI v4u ldw(rsrc_t rs, u32 voff, u32 soff) { return __builtin_amdgcn_raw_buffer_load_b128(rs, voff, soff, SC1); }
DI void stw(rsrc_t rs, v4u v, u32 voff, u32 soff) { __builtin_amdgcn_raw_buffer_store_b128(v, rs, voff + soff, 0, SC1); }
DI v4u lds128(LAS unsigned char* L, int off) { return *(LAS v4u*)(L + off); }
DI f32x4 ldsf4(LAS unsigned char* L, int off) { return *(LAS f32x4*)(L + off); }
DI v2u lds64(LAS unsigned char* L, int off) { return *(LAS v2u*)(L + off); }
DI v2u ldstr(LAS unsigned char* L, int off) {
  return __builtin_bit_cast(v2u, __builtin_amdgcn_ds_read_tr16_b64_v4i16((LAS s16x4*)(L + off)));
}
template <int N> DI void dma(rsrc_t rs, u32 voff, u32 soff, u32 ldsoff) {
  if constexpr (N == 4)
    asm volatile("s_mov_b32 m0, %0\n\ts_nop 0\n\tbuffer_load_dwordx4 %1, %2, %3 offen sc1 lds\n\tbuffer_load_dwordx4 %1, %2, %3 offen offset:1024 sc1 lds\n\t"
                 "buffer_load_dwordx4 %1, %2, %3 offen offset:2048 sc1 lds\n\tbuffer_load_dwordx4 %1, %2, %3 offen offset:3072 sc1 lds"
                 :: "s"(ldsoff), "v"(voff), "s"(rs), "s"(soff) : "memory");
  else
    asm volatile("s_mov_b32 m0, %0\n\ts_nop 0\n\tbuffer_load_dwordx4 %1, %2, %3 offen sc1 lds\n\tbuffer_load_dwordx4 %1, %2, %3 offen offset:1024 sc1 lds\n\t"
                 "buffer_load_dwordx4 %1, %2, %3 offen offset:2048 sc1 lds"
                 :: "s"(ldsoff), "v"(voff), "s"(rs), "s"(soff) : "memory");
}
DI void poll(unsigned char* ws, u32 off, int n, int lane, u64 T) {
  const GAS u64* f = (const GAS u64*)(ws + off + (lane < n ? lane : 0) * 128);
#pragma unroll 1
  for (int spins = 0; spins < SPIN_CAP; ++spins) {
    const u64 v = __hip_atomic_load(f, __ATOMIC_RELAXED, __HIP_MEMORY_SCOPE_AGENT);
    if (__all(v == T)) break;
    __builtin_amdgcn_s_sleep(1);
  }
  __builtin_amdgcn_fence(__ATOMIC_ACQUIRE, "wavefront");
}
DI void publish(unsigned char* ws, u32 off, u64 T) {
  __hip_atomic_store((GAS u64*)(ws + off), T, __ATOMIC_RELAXED, __HIP_MEMORY_SCOPE_AGENT);
}
DI void row_stats(const v4u (&r)[5], float& rstd, float& nb) {
  float s0 = 0.f, s1 = 0.f, q0 = 0.f, q1 = 0.f;
#pragma unroll
  for (int m = 0; m < 5; ++m) {
    s0 = dot2(r[m].x, 0x3f803f80u, s0); q0 = dot2(r[m].x, r[m].x, q0);
    s1 = dot2(r[m].y, 0x3f803f80u, s1); q1 = dot2(r[m].y, r[m].y, q1);
    s0 = dot2(r[m].z, 0x3f803f80u, s0); q0 = dot2(r[m].z, r[m].z, q0);
    s1 = dot2(r[m].w, 0x3f803f80u, s1); q1 = dot2(r[m].w, r[m].w, q1);
  }
  const float mean = row16_sum(s0 + s1) * (1.f / 640.f);
  const float var = fmaxf(row16_sum(q0 + q1) * (1.f / 640.f) - mean * mean, 0.f);
  rstd = __builtin_amdgcn_rsqf(var + 1e-5f);
  nb = -mean * rstd;
}

#define STAMP(i) asm volatile("; MARK " #i ::: "memory")

__global__ __launch_bounds__(NTHR, 2) void fused_block(const u16* __restrict__ x, const u16* __restrict__ wqkv, const u16* __restrict__ wo,
                                                       const u16* __restrict__ w1, const u16* __restrict__ b1, const u16* __restrict__ w2,
                                                       const u16* __restrict__ b2, const u16* __restrict__ cvec, const u16* __restrict__ wmod,
                                                       u16* __restrict__ out, unsigned char* ws) {
  extern __shared__ __attribute__((aligned(16))) unsigned char smem[];
  LAS unsigned char* L = (LAS unsigned char*)smem;
  const int tid = threadIdx.x, lane = tid & 63, b = blockIdx.x;
  const int w = __builtin_amdgcn_readfirstlane(tid >> 6);
  const int xcd = b & 7, jj = b >> 3;
  const int p = (xcd >> 1) * 8 + (jj >> 2), g = (xcd & 1) * 4 + (jj & 3);
  const rsrc_t rs = __builtin_amdgcn_make_buffer_rsrc(ws, 0, WS_USED, 0x00020000);
  const int g4 = lane >> 4, t16 = lane & 15;
  const u32 l16 = lane * 16;
  STAMP(0);

  const u64 Tv = __hip_atomic_load((const GAS u64*)(ws + OFF_E), __ATOMIC_RELAXED, __HIP_MEMORY_SCOPE_AGENT) + 1;
  const int job = w * 256 + b;
  const bool hasjob = job < 1612;
  const GAS unsigned char* jsrc; u32 jstride, jdst, jA, jB; int ppr; bool paired;
  if (job < 480) {
    const int gg = job / 60, rem = job % 60, part = rem / 20, j = rem % 20;
    jsrc = (const GAS unsigned char*)wqkv + (size_t)(32 * j) * 3840 + part * 1280 + gg * 160;
    jstride = 3840; ppr = 10; paired = false;
    jdst = OFF_WQ + (gg * 15 + part * 5) * 20480 + j * 1024; jA = 40960; jB = 20480;
  } else if (job < 640) {
    const int id = job - 480, gg = id / 20, j = id % 20;
    jsrc = (const GAS unsigned char*)wo + (size_t)(32 * j) * 1280 + gg * 160;
    jstride = 1280; ppr = 10; paired = true;
    jdst = OFF_WO + (gg * 20 + j) * 5120; jA = 2048; jB = 1024;
  } else if (job < 1072) {
    const int id = job - 640, gg = id / 54, j = id % 54;
    jsrc = (const GAS unsigned char*)w2 + (size_t)(32 * j) * 1280 + gg * 160;
    jstride = 1280; ppr = 10; paired = true;
    jdst = OFF_W2 + (gg * 54 + j) * 5120; jA = 2048; jB = 1024;
  } else {
    const int id = hasjob ? job - 1072 : 0, jb2 = id / 20, j = id % 20;
    jsrc = (const GAS unsigned char*)w1 + (size_t)(32 * j) * 3456 + jb2 * 128;
    jstride = 3456; ppr = 8; paired = true;
    jdst = OFF_W1 + (2 * jb2 * 20 + j) * 2048; jA = 40960; jB = 1024;
  }
  v4u jr[5];
  if (hasjob) {
    const u32 magic = ppr == 10 ? 6554u : 8192u;
#pragma unroll
    for (int it = 0; it < 5; ++it) {
      u32 idx = it * 64 + lane;
      if (it == 4 && ppr == 8) idx = lane;
      const u32 row = (idx * magic) >> 16, pc = idx - row * ppr;
      jr[it] = *(const GAS v4u*)(jsrc + row * jstride + pc * 16);
    }
  }
  v4u gw[3]; float gs[3];
  const int cl = b % 60, ks = b / 60;
  if (b < 240) {
#pragma unroll
    for (int i = 0; i < 3; ++i) {
      const int rr = (lane >> 3) + 8 * i;
      const int k = 160 * ks + 20 * w + (rr < 20 ? rr : 0);
      gw[i] = *(const GAS v4u*)((const GAS unsigned char*)wmod + (size_t)k * 7680 + cl * 128 + (lane & 7) * 16);
      const float c = bf2f(*(const GAS u16*)((const GAS unsigned char*)cvec + k * 2));
      gs[i] = rr < 20 ? c : 0.f;
    }
  }
  v4u xr[3][5];
#pragma unroll
  for (int a = 0; a < 3; ++a) {
    if (a < 2 || w < 4) {
      int tok = 64 * p - 16 + 4 * (w + 8 * a) + g4;
      tok = tok < 0 ? 0 : tok;
#pragma unroll
      for (int m = 0; m < 5; ++m) xr[a][m] = *(const GAS v4u*)((const GAS unsigned char*)x + (u32)(tok * 1280 + (t16 + 16 * m) * 16));
    } else {
#pragma unroll
      for (int m = 0; m < 5; ++m) xr[a][m] = v4u{0, 0, 0, 0};
    }
  }
  u32 pf = 0;
  if (w == 7) {
    u64 pc; int len;
    asm volatile("fused_pf_begin:\n\ts_getpc_b64 %0\n\ts_mov_b32 %1, fused_pf_end-fused_pf_begin" : "=s"(pc), "=s"(len));
    const int o = (jj * 16 + t16) * 128;
    if (o < len) pf = *(const GAS u32*)(pc + (u32)o);
  }
  SCHED();
  if (b < 240) {
    float acc[8];
#pragma unroll
    for (int e = 0; e < 8; ++e) acc[e] = 0.f;
#pragma unroll
    for (int i = 0; i < 3; ++i) {
      const float c = gs[i];
      const float s = c * __builtin_amdgcn_rcpf(1.f + __expf(-c));
#pragma unroll
      for (int e = 0; e < 4; ++e) { acc[2 * e] += s * blo(gw[i][e]); acc[2 * e + 1] += s * bhi(gw[i][e]); }
    }
#pragma unroll
    for (int e = 0; e < 8; ++e) acc[e] = x32_sum(x16_sum(dpp_add<0x128>(acc[e])));
    if (lane < 8) {
      *(LAS f32x4*)(L + L_GV + w * 256 + lane * 32) = f32x4{acc[0], acc[1], acc[2], acc[3]};
      *(LAS f32x4*)(L + L_GV + w * 256 + lane * 32 + 16) = f32x4{acc[4], acc[5], acc[6], acc[7]};
    }
  }
  if (hasjob) {
    LAS unsigned char* sc = L + w * 5120;
#pragma unroll
    for (int it = 0; it < 5; ++it)
      if (it < 4 || ppr == 10) *(LAS v4u*)(sc + it * 1024 + l16) = jr[it];
    __builtin_amdgcn_fence(__ATOMIC_RELEASE, "wavefront");
    __builtin_amdgcn_wave_barrier();
    __builtin_amdgcn_fence(__ATOMIC_ACQUIRE, "wavefront");
    const int rowb = ppr * 16;
    const int q = t16 >> 2, pp = lane & 3;
    const int rbase = (8 * g4 + q) * rowb;
#pragma unroll
    for (int ct = 0; ct < 5; ++ct) {
      if (ct < 4 || ppr == 10) {
        const int cb = (paired && ct < 4) ? 64 * (ct >> 1) + 8 * (ct & 1) + 16 * pp : 32 * ct + 8 * pp;
        const v2u lo = ldstr(sc, rbase + cb), hi = ldstr(sc, rbase + 4 * rowb + cb);
        stw(rs, v4u{lo.x, lo.y, hi.x, hi.y}, l16, jdst + (ct >> 1) * jA + (ct & 1) * jB);
      }
    }
  }
  bar();
  if (b < 240 && w == 0) {
    float s = 0.f;
#pragma unroll
    for (int ww = 0; ww < 8; ++ww) s += *(LAS float*)(L + L_GV + ww * 256 + lane * 4);
    __builtin_amdgcn_raw_buffer_store_b32(__builtin_bit_cast(u32, s), rs, lane * 4, OFF_MODP + (ks * 3840 + cl * 64) * 4, SC1);
  }
  drain();
  asm volatile("" ::"v"(pf));
  bar();
  const u32 Tlo = __builtin_amdgcn_readfirstlane((u32)Tv), Thi = __builtin_amdgcn_readfirstlane((u32)(Tv >> 32));
  const u64 T = ((u64)Thi << 32) | Tlo;
  if (tid == 0) publish(ws, OFF_F0 + b * 128, T);
  STAMP(1);
#pragma unroll
  for (int a = 0; a < 3; ++a) {
    if (a < 2 || w < 4) {
      float rstd, nb;
      row_stats(xr[a], rstd, nb);
      const f32x2 r2 = {rstd, rstd}, n2 = {nb, nb};
#pragma unroll
      for (int m = 0; m < 5; ++m)
#pragma unroll
        for (int e = 0; e < 4; ++e) xr[a][m][e] = pk(fma2(unpk(xr[a][m][e]), r2, n2));
    }
  }
  STAMP(2);
  if (w < 4) poll(ws, OFF_F0 + w * 64 * 128, 64, lane, T);
  bar();
  STAMP(3);
  if (b == 0 && tid == 0) publish(ws, OFF_E, T);

  if (tid < 320) {
    f32x4 s = {0.f, 0.f, 0.f, 0.f};
    v4u pv[4];
#pragma unroll
    for (int k = 0; k < 4; ++k) pv[k] = ldw(rs, tid * 16, OFF_MODP + k * 15360);
#pragma unroll
    for (int k = 0; k < 4; ++k) s += __builtin_bit_cast(f32x4, pv[k]);
    if (tid >= 160) s += 1.f;
    *(LAS f32x4*)(L + L_VEC + tid * 16) = s;
  }
  constexpr int R1 = 8;
  v4u wr[R1][2];
  const u32 wq0 = OFF_WQ + (g * 15 + 2 * w) * 20480, wq1 = OFF_WQ + (g * 15 + (w < 7 ? 2 * w + 1 : 14)) * 20480;
#pragma unroll
  for (int j = 0; j < R1; ++j) { wr[j][0] = ldw(rs, l16 + (j & 3) * 1024, wq0 + (j >> 2) * 4096); wr[j][1] = ldw(rs, l16 + (j & 3) * 1024, wq1 + (j >> 2) * 4096); }
  SCHED();
  bar();
  f32x4 acc1[5][2];
#pragma unroll
  for (int i = 0; i < 5; ++i) { acc1[i][0] = f32x4{0.f, 0.f, 0.f, 0.f}; acc1[i][1] = f32x4{0.f, 0.f, 0.f, 0.f}; }
  const int rd_even = g4 * 256 + ((t16 ^ g4) * 16), rd_odd = rd_even ^ 64;
  auto prep1 = [&](int m) {
    const int k0 = (t16 + 16 * m) * 8;
    const f32x4 s0 = ldsf4(L, L_VEC + k0 * 4), s1 = ldsf4(L, L_VEC + k0 * 4 + 16);
    const f32x4 c0 = ldsf4(L, L_VEC + 2560 + k0 * 4), c1 = ldsf4(L, L_VEC + 2560 + k0 * 4 + 16);
#pragma unroll
    for (int a = 0; a < 3; ++a) {
      if (a < 2 || w < 4) {
        const int grp = w + 8 * a, tt = grp >> 2, t = 4 * (grp & 3) + g4;
        const v4u r = xr[a][m];
        v4u o;
        o.x = pk(fma2(unpk(r.x), c0.xy, s0.xy));
        o.y = pk(fma2(unpk(r.y), c0.zw, s0.zw));
        o.z = pk(fma2(unpk(r.z), c1.xy, s1.xy));
        o.w = pk(fma2(unpk(r.w), c1.zw, s1.zw));
        *(LAS v4u*)(L + L_A + tt * 20480 + (4 * m + (t16 >> 2)) * 1024 + (t16 & 3) * 256 + ((t ^ (t16 & 7)) * 16)) = o;
      }
    }
  };
  prep1(0);
  bar();
  STAMP(4);
#pragma unroll
  for (int m = 0; m < 5; ++m) {
    if (m < 4) prep1(m + 1);
#pragma unroll
    for (int q = 0; q < 4; ++q) {
      const int j = 4 * m + q;
      const v4u wa = wr[j % R1][0], wb = wr[j % R1][1];
      if (j + R1 < 20) {
        wr[j % R1][0] = ldw(rs, l16 + ((j + R1) & 3) * 1024, wq0 + ((j + R1) >> 2) * 4096);
        wr[j % R1][1] = ldw(rs, l16 + ((j + R1) & 3) * 1024, wq1 + ((j + R1) >> 2) * 4096);
      }
#pragma unroll
      for (int tt = 0; tt < 5; ++tt) {
        const v4u a = lds128(L, L_A + tt * 20480 + j * 1024 + ((j & 1) ? rd_odd : rd_even));
        acc1[tt][0] = mfma(wa, a, acc1[tt][0]);
        acc1[tt][1] = mfma(wb, a, acc1[tt][1]);
      }
      SCHED();
    }
    bar();
  }
  STAMP(5);
#pragma unroll
  for (int tt = 0; tt < 5; ++tt)
#pragma unroll
    for (int c = 0; c < 2; ++c)
      if (c == 0 || w < 7)
        *(LAS v2u*)(L + L_A + (tt * 16 + t16) * QS + (2 * w + c) * 32 + g4 * 8) =
            v2u{pack2(acc1[tt][c].x, acc1[tt][c].y), pack2(acc1[tt][c].z, acc1[tt][c].w)};
  bar();
  STAMP(6);
  {
    const int h = w & 3, par = w >> 2;
    const float cs = 0.22360679774997896f * 1.4426950408889634f;
#pragma unroll
    for (int qt = 0; qt < 2; ++qt) {
      const int qrow = 2 * (8 + 16 * qt + t16) + par;
      const int qo = L_A + qrow * QS + 40 * h + (g4 < 3 ? 16 * g4 : 0);
      v2u qlo = lds64(L, qo), qhi = lds64(L, qo + 8);
      if (g4 == 3) qlo = v2u{0, 0};
      if (g4 >= 2) qhi = v2u{0, 0};
      const v4u qf = {qlo.x, qlo.y, qhi.x, qhi.y};
      f32x4 sc[2];
#pragma unroll
      for (int kt = 0; kt < 2; ++kt) {
        int ki = 16 * qt + 16 * kt + t16;
        ki = ki > 39 ? 39 : ki;
        const int ko = L_A + (2 * ki + par) * QS + 160 + 40 * h + 16 * g4;
        const v2u klo = lds64(L, ko), khi = lds64(L, ko + 8);
        sc[kt] = mfma(v4u{klo.x, klo.y, khi.x, khi.y}, qf, f32x4{0.f, 0.f, 0.f, 0.f});
      }
      float mx = -3.0e38f;
#pragma unroll
      for (int kt = 0; kt < 2; ++kt)
#pragma unroll
        for (int r = 0; r < 4; ++r) {
          const int kr = 16 * kt + 4 * g4 + r;
          bool ok = (unsigned)(kr - t16 - 1) < 8u;
          if (qt == 0) ok = ok && (p > 0 || kr >= 8);
          sc[kt][r] = ok ? sc[kt][r] : -3.0e38f;
          mx = fmaxf(mx, sc[kt][r]);
        }
      mx = x32_max(x16_max(mx));
      float sum = 0.f;
#pragma unroll
      for (int kt = 0; kt < 2; ++kt)
#pragma unroll
        for (int r = 0; r < 4; ++r) { sc[kt][r] = __builtin_amdgcn_exp2f((sc[kt][r] - mx) * cs); sum += sc[kt][r]; }
      sum = x32_sum(x16_sum(sum));
      const float inv = __builtin_amdgcn_rcpf(sum);
      const v4u pf4 = {pack2(sc[0].x * inv, sc[0].y * inv), pack2(sc[0].z * inv, sc[0].w * inv), pack2(sc[1].x * inv, sc[1].y * inv), pack2(sc[1].z * inv, sc[1].w * inv)};
      const int vq = t16 >> 2, vp = lane & 3;
      int ka = 16 * qt + 4 * g4 + vq, kb = ka + 16;
      kb = kb > 39 ? 39 : kb;
      const int va = L_A + (2 * ka + par) * QS + 320 + 40 * h, vb = L_A + (2 * kb + par) * QS + 320 + 40 * h;
      const v2u a0 = ldstr(L, va + 8 * vp), b0 = ldstr(L, vb + 8 * vp);
      const v2u a1 = ldstr(L, va + 32), b1v = ldstr(L, vb + 32);
      const f32x4 o0 = mfma(v4u{a0.x, a0.y, b0.x, b0.y}, pf4, f32x4{0.f, 0.f, 0.f, 0.f});
      const f32x4 o1 = mfma(v4u{a1.x, a1.y, b1v.x, b1v.y}, pf4, f32x4{0.f, 0.f, 0.f, 0.f});
      const int orow = L_OB + (par * 32 + 16 * qt + t16) * OS + 40 * h;
      *(LAS v2u*)(L + orow + 8 * g4) = v2u{pack2(o0.x, o0.y), pack2(o0.z, o0.w)};
      if (g4 == 0) *(LAS v2u*)(L + orow + 32) = v2u{pack2(o1.x, o1.y), pack2(o1.z, o1.w)};
    }
  }
  bar();
  {
    const int tok = 2 * (lane & 31) + (lane >> 5);
#pragma unroll
    for (int i = 0; i < 2; ++i) {
      const int u = w + 8 * i;
      if (u < 10) {
        const int ch = 10 * g + u;
        const v4u v = lds128(L, L_OB + lane * OS + 16 * u);
        stw(rs, v, (tok >> 4) * 20480 + (tok & 15) * 16, OFF_O + p * 81920 + (ch >> 2) * 1024 + (ch & 3) * 256);
      }
    }
  }
  drain();
  bar();
  if (tid == 0) publish(ws, OFF_F1 + (p * 8 + g) * 128, T);
  STAMP(7);

  const int s4 = w & 3, kh = w >> 2;
#pragma unroll
  for (int i = 0; i < 4; ++i) {
    const int gi = w + 8 * i;
    if (gi < 25) dma<4>(rs, l16, OFF_WO + g * 102400 + gi * 4096, L_A + gi * 4096);
  }
  const int tokg = 64 * p + 16 * s4 + t16;
  const int fo = kh == 0 ? 8 * g4 : 32 + 8 * g4;
  const v4u xa = *(const GAS v4u*)((const GAS unsigned char*)x + (u32)(tokg * 1280 + (80 * g + fo) * 2));
  const v2u xb = *(const GAS v2u*)((const GAS unsigned char*)x + (u32)(tokg * 1280 + (80 * g + 64 + 4 * g4) * 2));
  if (tid < 320) {
    f32x4 s = {0.f, 0.f, 0.f, 0.f};
    v4u pv[4];
#pragma unroll
    for (int k = 0; k < 4; ++k) pv[k] = ldw(rs, tid * 16, OFF_MODP + k * 15360 + 1920 * 4);
#pragma unroll
    for (int k = 0; k < 4; ++k) s += __builtin_bit_cast(f32x4, pv[k]);
    if (tid >= 160) s += 1.f;
    *(LAS f32x4*)(L + L_VEC + 5120 + tid * 16) = s;
  } else if (tid < 360) {
    const int i = tid - 320, which = i / 20, c4 = i % 20;
    f32x4 s = {0.f, 0.f, 0.f, 0.f};
    v4u pv[4];
#pragma unroll
    for (int k = 0; k < 4; ++k) pv[k] = ldw(rs, (which ? 3200 : 1280) * 4 + c4 * 16, OFF_MODP + k * 15360 + g * 320);
#pragma unroll
    for (int k = 0; k < 4; ++k) s += __builtin_bit_cast(f32x4, pv[k]);
    *(LAS f32x4*)(L + L_SM + which * 320 + c4 * 16) = s;
  } else if (tid >= 384 && tid < 404) {
    const int c4 = tid - 384;
    const v2u v = *(const GAS v2u*)((const GAS unsigned char*)b2 + (80 * g + 4 * c4) * 2);
    *(LAS f32x4*)(L + L_SM + 640 + c4 * 16) = f32x4{blo(v.x), bhi(v.x), blo(v.y), bhi(v.y)};
  }
  if (w == 7) poll(ws, OFF_F1 + p * 8 * 128, 8, lane, T);
  bar();
  STAMP(8);
  v4u oa[10];
#pragma unroll
  for (int i = 0; i < 10; ++i) oa[i] = ldw(rs, l16 + (i & 3) * 1024, OFF_O + (p * 4 + s4) * 20480 + kh * 10240 + (i >> 2) * 4096);
  asm volatile("s_waitcnt vmcnt(10)" ::: "memory");
  bar();
  f32x4 acc2[5];
#pragma unroll
  for (int i = 0; i < 5; ++i) acc2[i] = f32x4{0.f, 0.f, 0.f, 0.f};
#pragma unroll
  for (int i = 0; i < 10; ++i) {
#pragma unroll
    for (int ct = 0; ct < 5; ++ct) acc2[ct] = mfma(lds128(L, L_A + ((10 * kh + i) * 5 + ct) * 1024 + l16), oa[i], acc2[ct]);
  }
  if (kh == 0) {
    *(LAS f32x4*)(L + L_RED + (s4 * 5 + 2) * 1024 + l16) = acc2[2];
    *(LAS f32x4*)(L + L_RED + (s4 * 5 + 3) * 1024 + l16) = acc2[3];
  } else {
    *(LAS f32x4*)(L + L_RED + (s4 * 5 + 0) * 1024 + l16) = acc2[0];
    *(LAS f32x4*)(L + L_RED + (s4 * 5 + 1) * 1024 + l16) = acc2[1];
    *(LAS f32x4*)(L + L_RED + (s4 * 5 + 4) * 1024 + l16) = acc2[4];
  }
  bar();
  STAMP(9);
  v4u x1a; v2u x1b = {0, 0};
  {
    const int c0 = kh == 0 ? 0 : 2;
    const f32x4 u0 = (kh == 0 ? acc2[0] : acc2[2]) + ldsf4(L, L_RED + (s4 * 5 + c0) * 1024 + l16);
    const f32x4 u1 = (kh == 0 ? acc2[1] : acc2[3]) + ldsf4(L, L_RED + (s4 * 5 + c0 + 1) * 1024 + l16);
    const f32x4 g0 = ldsf4(L, L_SM + fo * 4), g1 = ldsf4(L, L_SM + fo * 4 + 16);
    x1a.x = pack2(blo(xa.x) + g0.x * u0.x, bhi(xa.x) + g0.y * u0.y);
    x1a.y = pack2(blo(xa.y) + g0.z * u0.z, bhi(xa.y) + g0.w * u0.w);
    x1a.z = pack2(blo(xa.z) + g1.x * u1.x, bhi(xa.z) + g1.y * u1.y);
    x1a.w = pack2(blo(xa.w) + g1.z * u1.z, bhi(xa.w) + g1.w * u1.w);
    stw(rs, x1a, tokg * 1280 + (80 * g + fo) * 2, OFF_X1);
    if (kh == 0) {
      const f32x4 u2 = acc2[4] + ldsf4(L, L_RED + (s4 * 5 + 4) * 1024 + l16);
      const f32x4 g2 = ldsf4(L, L_SM + (64 + 4 * g4) * 4);
      x1b.x = pack2(blo(xb.x) + g2.x * u2.x, bhi(xb.x) + g2.y * u2.y);
      x1b.y = pack2(blo(xb.y) + g2.z * u2.z, bhi(xb.y) + g2.w * u2.w);
      __builtin_amdgcn_raw_buffer_store_b64(x1b, rs, tokg * 1280 + (80 * g + 64 + 4 * g4) * 2, OFF_X1, SC1);
    }
  }
  drain();
  bar();
  if (tid == 0) publish(ws, OFF_F2 + (p * 8 + g) * 128, T);
  STAMP(10);

  const int j0 = g < 7 ? 7 * g : 48, nk = g < 6 ? 7 : 6;
  const bool act3 = w < nk;
  const int jb = j0 + (act3 ? w : 0);
  constexpr int R3 = 8;
  v4u w3[R3][2];
  const u32 w1o = OFF_W1 + jb * 40960;
#pragma unroll
  for (int j = 0; j < R3; ++j) { w3[j][0] = ldw(rs, l16 + (j & 1) * 2048, w1o + (j >> 1) * 4096); w3[j][1] = ldw(rs, l16 + (j & 1) * 2048 + 1024, w1o + (j >> 1) * 4096); }
  const v4u b1v = *(const GAS v4u*)((const GAS unsigned char*)b1 + (32 * jb + 8 * g4) * 2);
  SCHED();
  if (w == 7) poll(ws, OFF_F2 + p * 8 * 128, 8, lane, T);
  bar();
  STAMP(11);
  v4u yr[2][5];
#pragma unroll
  for (int a = 0; a < 2; ++a)
#pragma unroll
    for (int m = 0; m < 5; ++m) yr[a][m] = ldw(rs, (64 * p + 8 * w + 4 * a + g4) * 1280 + (t16 + 16 * m) * 16, OFF_X1);
  SCHED();
#pragma unroll
  for (int a = 0; a < 2; ++a) {
    asm volatile("" ::: "memory");
    SCHED();
    float rstd, nb;
    row_stats(yr[a], rstd, nb);
    const f32x2 r2 = {rstd, rstd}, n2 = {nb, nb};
#pragma unroll
    for (int m = 0; m < 5; ++m) {
      const int k0 = (t16 + 16 * m) * 8;
      const f32x4 s0 = ldsf4(L, L_VEC + 5120 + k0 * 4), s1 = ldsf4(L, L_VEC + 5120 + k0 * 4 + 16);
      const f32x4 c0 = ldsf4(L, L_VEC + 7680 + k0 * 4), c1 = ldsf4(L, L_VEC + 7680 + k0 * 4 + 16);
      yr[a][m].x = pk(fma2(fma2(unpk(yr[a][m].x), r2, n2), c0.xy, s0.xy));
      yr[a][m].y = pk(fma2(fma2(unpk(yr[a][m].y), r2, n2), c0.zw, s0.zw));
      yr[a][m].z = pk(fma2(fma2(unpk(yr[a][m].z), r2, n2), c1.xy, s1.xy));
      yr[a][m].w = pk(fma2(fma2(unpk(yr[a][m].w), r2, n2), c1.zw, s1.zw));
    }
    row_stats(yr[a], rstd, nb);
    const f32x2 r3 = {rstd, rstd}, n3 = {nb, nb};
    const int row = 8 * w + 4 * a + g4, tt = row >> 4, t = row & 15;
#pragma unroll
    for (int m = 0; m < 5; ++m) {
      const v4u o = {pk(fma2(unpk(yr[a][m].x), r3, n3)), pk(fma2(unpk(yr[a][m].y), r3, n3)),
                     pk(fma2(unpk(yr[a][m].z), r3, n3)), pk(fma2(unpk(yr[a][m].w), r3, n3))};
      *(LAS v4u*)(L + L_A + tt * 20480 + (4 * m + (t16 >> 2)) * 1024 + (t16 & 3) * 256 + ((t ^ (t16 & 7)) * 16)) = o;
    }
  }
  bar();
  STAMP(12);
  f32x4 acc3[4][2];
#pragma unroll
  for (int i = 0; i < 4; ++i) { acc3[i][0] = f32x4{0.f, 0.f, 0.f, 0.f}; acc3[i][1] = f32x4{0.f, 0.f, 0.f, 0.f}; }
  if (act3) {
#pragma unroll
    for (int j = 0; j < 20; ++j) {
      const v4u wa = w3[j % R3][0], wb = w3[j % R3][1];
      if (j + R3 < 20) {
        w3[j % R3][0] = ldw(rs, l16 + ((j + R3) & 1) * 2048, w1o + ((j + R3) >> 1) * 4096);
        w3[j % R3][1] = ldw(rs, l16 + ((j + R3) & 1) * 2048 + 1024, w1o + ((j + R3) >> 1) * 4096);
      }
#pragma unroll
      for (int tt = 0; tt < 4; ++tt) {
        const v4u a = lds128(L, L_A + tt * 20480 + j * 1024 + ((j & 1) ? rd_odd : rd_even));
        acc3[tt][0] = mfma(wa, a, acc3[tt][0]);
        acc3[tt][1] = mfma(wb, a, acc3[tt][1]);
      }
      SCHED();
    }
  }
  bar();
  STAMP(13);
  if (act3) {
    const float bb[8] = {blo(b1v.x), bhi(b1v.x), blo(b1v.y), bhi(b1v.y), blo(b1v.z), bhi(b1v.z), blo(b1v.w), bhi(b1v.w)};
    const float k1 = -2.f * 0.7978845608028654f * 1.4426950408889634f, k3 = k1 * 0.044715f;
#pragma unroll
    for (int tt = 0; tt < 4; ++tt) {
      float hv[8];
#pragma unroll
      for (int e = 0; e < 8; ++e) {
        const float v = acc3[tt][e >> 2][e & 3] + bb[e];
        const float ex = __builtin_amdgcn_exp2f(v * (k1 + k3 * v * v));
        hv[e] = v * __builtin_amdgcn_rcpf(1.f + ex);
      }
      stw(rs, v4u{pack2(hv[0], hv[1]), pack2(hv[2], hv[3]), pack2(hv[4], hv[5]), pack2(hv[6], hv[7])}, l16, OFF_HH + ((p * 4 + tt) * 54 + jb) * 1024);
    }
  }
  drain();
  bar();
  if (tid == 0) publish(ws, OFF_F3 + (p * 8 + g) * 128, T);
  STAMP(14);

  const u32 w2o = OFF_W2 + (g * 54 + 27 * kh) * 5120;
  const u32 ring = kh * 76800;
#pragma unroll
  for (int i = 0; i < 5; ++i) {
    const int gi = s4 + 4 * i;
    if (gi < 18) dma<4>(rs, l16, w2o + gi * 4096, ring + gi * 4096);
    else if (gi == 18) dma<3>(rs, l16, w2o + gi * 4096, ring + gi * 4096);
  }
  if (w == 7) poll(ws, OFF_F3 + p * 8 * 128, 8, lane, T);
  bar();
  STAMP(15);
  v4u ha[27];
#pragma unroll
  for (int i = 0; i < 27; ++i) ha[i] = ldw(rs, l16 + (i & 3) * 1024, OFF_HH + ((p * 4 + s4) * 54 + 27 * kh) * 1024 + (i >> 2) * 4096);
  asm volatile("s_waitcnt vmcnt(27)" ::: "memory");
  bar();
  f32x4 acc4[5];
#pragma unroll
  for (int i = 0; i < 5; ++i) acc4[i] = f32x4{0.f, 0.f, 0.f, 0.f};
#pragma unroll
  for (int n = 0; n < 9; ++n) {
#pragma unroll
    for (int q = 0; q < 3; ++q) {
      const int i = 3 * n + q;
#pragma unroll
      for (int ct = 0; ct < 5; ++ct) acc4[ct] = mfma(lds128(L, ring + ((i % 15) * 5 + ct) * 1024 + l16), ha[i], acc4[ct]);
    }
    if (n == 4) asm volatile("s_waitcnt vmcnt(9)" ::: "memory");
    if (n == 5) asm volatile("s_waitcnt vmcnt(6)" ::: "memory");
    if (n == 6) asm volatile("s_waitcnt vmcnt(3)" ::: "memory");
    if (n == 7) asm volatile("s_waitcnt vmcnt(0)" ::: "memory");
    bar();
    if (n < 4) {
      if (s4 < 3) dma<4>(rs, l16, w2o + (75 + 15 * n + 4 * s4) * 1024, ring + (15 * n + 4 * s4) * 1024);
      else dma<3>(rs, l16, w2o + (75 + 15 * n + 12) * 1024, ring + (15 * n + 12) * 1024);
    }
  }
  STAMP(16);
  if (kh == 0) {
    *(LAS f32x4*)(L + L_RED + (s4 * 5 + 2) * 1024 + l16) = acc4[2];
    *(LAS f32x4*)(L + L_RED + (s4 * 5 + 3) * 1024 + l16) = acc4[3];
  } else {
    *(LAS f32x4*)(L + L_RED + (s4 * 5 + 0) * 1024 + l16) = acc4[0];
    *(LAS f32x4*)(L + L_RED + (s4 * 5 + 1) * 1024 + l16) = acc4[1];
    *(LAS f32x4*)(L + L_RED + (s4 * 5 + 4) * 1024 + l16) = acc4[4];
  }
  bar();
  {
    const int c0 = kh == 0 ? 0 : 2;
    const f32x4 u0 = (kh == 0 ? acc4[0] : acc4[2]) + ldsf4(L, L_RED + (s4 * 5 + c0) * 1024 + l16);
    const f32x4 u1 = (kh == 0 ? acc4[1] : acc4[3]) + ldsf4(L, L_RED + (s4 * 5 + c0 + 1) * 1024 + l16);
    const f32x4 g0 = ldsf4(L, L_SM + 320 + fo * 4), g1 = ldsf4(L, L_SM + 320 + fo * 4 + 16);
    const f32x4 e0 = ldsf4(L, L_SM + 640 + fo * 4), e1 = ldsf4(L, L_SM + 640 + fo * 4 + 16);
    v4u o;
    o.x = pack2(blo(x1a.x) + g0.x * (u0.x + e0.x), bhi(x1a.x) + g0.y * (u0.y + e0.y));
    o.y = pack2(blo(x1a.y) + g0.z * (u0.z + e0.z), bhi(x1a.y) + g0.w * (u0.w + e0.w));
    o.z = pack2(blo(x1a.z) + g1.x * (u1.x + e1.x), bhi(x1a.z) + g1.y * (u1.y + e1.y));
    o.w = pack2(blo(x1a.w) + g1.z * (u1.z + e1.z), bhi(x1a.w) + g1.w * (u1.w + e1.w));
    *(GAS v4u*)((GAS unsigned char*)out + (u32)(tokg * 1280 + (80 * g + fo) * 2)) = o;
    if (kh == 0) {
      const f32x4 u2 = acc4[4] + ldsf4(L, L_RED + (s4 * 5 + 4) * 1024 + l16);
      const f32x4 g2 = ldsf4(L, L_SM + 320 + (64 + 4 * g4) * 4), e2 = ldsf4(L, L_SM + 640 + (64 + 4 * g4) * 4);
      const v2u o2 = {pack2(blo(x1b.x) + g2.x * (u2.x + e2.x), bhi(x1b.x) + g2.y * (u2.y + e2.y)),
                      pack2(blo(x1b.y) + g2.z * (u2.z + e2.z), bhi(x1b.y) + g2.w * (u2.w + e2.w))};
      *(GAS v2u*)((GAS unsigned char*)out + (u32)(tokg * 1280 + (80 * g + 64 + 4 * g4) * 2)) = o2;
    }
  }
  STAMP(17);
  if (tid == 0) publish(ws, OFF_DIAG + b * 8, __builtin_amdgcn_s_memrealtime());
  asm volatile("fused_pf_end:" ::: "memory");
}

__global__ void k_probe(unsigned char* ws) {
  const u64 t = __builtin_amdgcn_s_memrealtime();
  u64 m = 0;
  for (int i = 0; i < 4; ++i) { const u64 v = *(const GAS u64*)(ws + OFF_DIAG + (threadIdx.x * 4 + i) * 8); m = v > m ? v : m; }
  for (int o = 32; o > 0; o >>= 1) { const u64 v = __shfl_xor(m, o); m = v > m ? v : m; }
  const u64 d = t > m ? t - m : 0;
  const u64 until = __builtin_amdgcn_s_memrealtime() + 0 * (d < 100000 ? d : 0);
  while (__builtin_amdgcn_s_memrealtime() < until) __builtin_amdgcn_s_sleep(2);
}
extern "C" void kernel_launch(void* const* d_in, const int* in_sizes, int n_in, void* d_out, int out_size, void* d_ws, size_t ws_size,
                              hipStream_t stream) {
  (void)in_sizes; (void)n_in; (void)out_size; (void)ws_size;
  (void)hipFuncSetAttribute((const void*)fused_block, hipFuncAttributeMaxDynamicSharedMemorySize, LDS_BYTES);
  fused_block<<<NWG, NTHR, LDS_BYTES, stream>>>((const u16*)d_in[0], (const u16*)d_in[1], (const u16*)d_in[2], (const u16*)d_in[4],
                                                (const u16*)d_in[5], (const u16*)d_in[6], (const u16*)d_in[7], (const u16*)d_in[8],
                                                (const u16*)d_in[9], (u16*)d_out, (unsigned char*)d_ws);
  k_probe<<<1, 64, 0, stream>>>((unsigned char*)d_ws);
}
```

```cpp
#include <hip/hip_runtime.h>

typedef unsigned short u16;
typedef unsigned int u32;
typedef unsigned long long u64;
typedef u32 v4u __attribute__((ext_vector_type(4)));
typedef u32 v2u __attribute__((ext_vector_type(2)));
typedef float f32x4 __attribute__((ext_vector_type(4)));
typedef float f32x2 __attribute__((ext_vector_type(2)));
typedef short s16x8 __attribute__((ext_vector_type(8)));
typedef short s16x4 __attribute__((ext_vector_type(4)));
typedef __bf16 bf16x2 __attribute__((ext_vector_type(2)));
#define GAS __attribute__((address_space(1)))
#define LAS __attribute__((address_space(3)))
#define DI __device__ __forceinline__
typedef __amdgpu_buffer_rsrc_t rsrc_t;

#define XN 2

constexpr int NWG = 256, NTHR = 512;
constexpr u32 OFF_E = 0, OFF_F0 = 0x1000, OFF_F1 = 0xA000, OFF_F2 = 0x12000, OFF_F3 = 0x1A000;
constexpr u32 OFF_MODP = 0x30000, OFF_WQ = 0x40000, OFF_WO = 0x300000, OFF_W1 = 0x400000, OFF_W2 = 0x700000;
constexpr u32 OFF_O = 0xA00000, OFF_X1 = 0xD00000, OFF_HH = 0x1000000, WS_USED = 0x1800000;
constexpr u32 OFF_DIAG = 0x1800000;
constexpr int L_A = 0, L_OB = 40960, L_GV = 40960, L_RED = 102400, L_SM = 153600, L_VEC = 154624, LDS_BYTES = 163840;
constexpr int QS = 488;
constexpr int OS = 176;
constexpr int SPIN_CAP = 1 << 14;
constexpr int SC1 = 16;

DI f32x4 mfma(v4u a, v4u b, f32x4 c) {
  return __builtin_amdgcn_mfma_f32_16x16x32_bf16(__builtin_bit_cast(s16x8, a), __builtin_bit_cast(s16x8, b), c, 0, 0, 0);
}
DI u32 pack2(float a, float b) { f32x2 v = {a, b}; return __builtin_bit_cast(u32, __builtin_convertvector(v, bf16x2)); }
DI float blo(u32 u) { return __builtin_bit_cast(float, u << 16); }
DI float bhi(u32 u) { return __builtin_bit_cast(float, u & 0xffff0000u); }
DI f32x2 unpk(u32 u) { return f32x2{blo(u), bhi(u)}; }
DI f32x2 fma2(f32x2 a, f32x2 b, f32x2 c) { return __builtin_elementwise_fma(a, b, c); }
DI u32 pk(f32x2 v) { return __builtin_bit_cast(u32, __builtin_convertvector(v, bf16x2)); }
DI float dot2(u32 a, u32 b, float c) { return __builtin_amdgcn_fdot2_f32_bf16(__builtin_bit_cast(bf16x2, a), __builtin_bit_cast(bf16x2, b), c, false); }
DI float bf2f(u16 v) { return __builtin_bit_cast(float, (u32)v << 16); }
template <int CTRL> DI float dpp_add(float v) {
  return v + __builtin_bit_cast(float, __builtin_amdgcn_update_dpp(0, __builtin_bit_cast(int, v), CTRL, 0xf, 0xf, true));
}
DI float row16_sum(float v) { v = dpp_add<0x128>(v); v = dpp_add<0x124>(v); v = dpp_add<0x122>(v); return dpp_add<0x121>(v); }
DI float x16_sum(float v) {
  auto r = __builtin_amdgcn_permlane16_swap(__builtin_bit_cast(u32, v), __builtin_bit_cast(u32, v), false, false);
  const u32 a = r[0], c = r[1];
  return __builtin_bit_cast(float, a) + __builtin_bit_cast(float, c);
}
DI float x32_sum(float v) {
  auto r = __builtin_amdgcn_permlane32_swap(__builtin_bit_cast(u32, v), __builtin_bit_cast(u32, v), false, false);
  const u32 a = r[0], c = r[1];
  return __builtin_bit_cast(float, a) + __builtin_bit_cast(float, c);
}
DI float x16_max(float v) {
  auto r = __builtin_amdgcn_permlane16_swap(__builtin_bit_cast(u32, v), __builtin_bit_cast(u32, v), false, false);
  const u32 a = r[0], c = r[1];
  return fmaxf(__builtin_bit_cast(float, a), __builtin_bit_cast(float, c));
}
DI float x32_max(float v) {
  auto r = __builtin_amdgcn_permlane32_swap(__builtin_bit_cast(u32, v), __builtin_bit_cast(u32, v), false, false);
  const u32 a = r[0], c = r[1];
  return fmaxf(__builtin_bit_cast(float, a), __builtin_bit_cast(float, c));
}
DI void bar() { asm volatile("s_waitcnt lgkmcnt(0)\n\ts_barrier" ::: "memory"); }
DI void drain() { asm volatile("s_waitcnt vmcnt(0)" ::: "memory"); }
#define SCHED() __builtin_amdgcn_sched_barrier(0)
#define SGB(mask, n) __builtin_amdgcn_sched_group_barrier(mask, n, 0)
#define VMCNT(n) asm volatile("s_waitcnt vmcnt(" #n ")" ::: "memory")
DI v4u ldw(rsrc_t rs, u32 voff, u32 soff) { return __builtin_amdgcn_raw_buffer_load_b128(rs, voff, soff, SC1); }
DI void stw(rsrc_t rs, v4u v, u32 voff, u32 soff) { __builtin_amdgcn_raw_buffer_store_b128(v, rs, voff + soff, 0, SC1); }
DI v4u lds128(LAS unsigned char* L, int off) { return *(LAS v4u*)(L + off); }
DI f32x4 ldsf4(LAS unsigned char* L, int off) { return *(LAS f32x4*)(L + off); }
DI v2u lds64(LAS unsigned char* L, int off) { return *(LAS v2u*)(L + off); }
DI v2u ldstr(LAS unsigned char* L, int off) {
  return __builtin_bit_cast(v2u, __builtin_amdgcn_ds_read_tr16_b64_v4i16((LAS s16x4*)(L + off)));
}
template <int N> DI void dma(rsrc_t rs, u32 voff, u32 soff, u32 ldsoff) {
#define DMA_I(o) "buffer_load_dwordx4 %1, %2, %3 offen offset:" #o " sc1 lds\n\t"
#define DMA_ARGS :: "s"(ldsoff), "v"(voff), "s"(rs), "s"(soff) : "memory"
  if constexpr (N == 4) asm volatile("s_mov_b32 m0, %0\n\ts_nop 0\n\t" DMA_I(0) DMA_I(1024) DMA_I(2048) DMA_I(3072) DMA_ARGS);
  else if constexpr (N == 3) asm volatile("s_mov_b32 m0, %0\n\ts_nop 0\n\t" DMA_I(0) DMA_I(1024) DMA_I(2048) DMA_ARGS);
  else if constexpr (N == 2) asm volatile("s_mov_b32 m0, %0\n\ts_nop 0\n\t" DMA_I(0) DMA_I(1024) DMA_ARGS);
  else asm volatile("s_mov_b32 m0, %0\n\ts_nop 0\n\t" DMA_I(0) DMA_ARGS);
}
DI void poll(unsigned char* ws, u32 off, int n, int lane, u64 T) {
  const GAS u64* f = (const GAS u64*)(ws + off + (lane < n ? lane : 0) * 128);
#pragma unroll 1
  for (int spins = 0; spins < SPIN_CAP; ++spins) {
    const u64 v = __hip_atomic_load(f, __ATOMIC_RELAXED, __HIP_MEMORY_SCOPE_AGENT);
    if (__all(v == T)) break;
    __builtin_amdgcn_s_sleep(1);
  }
  __builtin_amdgcn_fence(__ATOMIC_ACQUIRE, "wavefront");
}
DI void publish(unsigned char* ws, u32 off, u64 T) {
  __hip_atomic_store((GAS u64*)(ws + off), T, __ATOMIC_RELAXED, __HIP_MEMORY_SCOPE_AGENT);
}
DI void row_stats(const v4u (&r)[5], float& rstd, float& nb) {
  float s0 = 0.f, s1 = 0.f, q0 = 0.f, q1 = 0.f;
#pragma unroll
  for (int m = 0; m < 5; ++m) {
    s0 = dot2(r[m].x, 0x3f803f80u, s0); q0 = dot2(r[m].x, r[m].x, q0);
    s1 = dot2(r[m].y, 0x3f803f80u, s1); q1 = dot2(r[m].y, r[m].y, q1);
    s0 = dot2(r[m].z, 0x3f803f80u, s0); q0 = dot2(r[m].z, r[m].z, q0);
    s1 = dot2(r[m].w, 0x3f803f80u, s1); q1 = dot2(r[m].w, r[m].w, q1);
  }
  const float mean = row16_sum(s0 + s1) * (1.f / 640.f);
  const float var = fmaxf(row16_sum(q0 + q1) * (1.f / 640.f) - mean * mean, 0.f);
  rstd = __builtin_amdgcn_rsqf(var + 1e-5f);
  nb = -mean * rstd;
}

#define STAMP(i) asm volatile("; MARK " #i ::: "memory")

typedef const GAS unsigned char* gptr;
__global__ __launch_bounds__(NTHR, 2) void fused_block(const u16* a_x, const u16* a_wqkv, const u16* a_wo, const u16* a_w1, const u16* a_b1,
                                                       const u16* a_w2, const u16* a_b2, const u16* a_c, const u16* a_wmod, u16* a_out,
                                                       unsigned char* a_ws) {
  extern __shared__ __attribute__((aligned(16))) unsigned char smem[];
  const __attribute__((address_space(4))) u64* ka = (const __attribute__((address_space(4))) u64*)__builtin_amdgcn_kernarg_segment_ptr();
  u64 k0 = ka[0], k1 = ka[1], k2 = ka[2], k3 = ka[3], k4 = ka[4], k5 = ka[5], k6 = ka[6], k7 = ka[7], k8 = ka[8], k9 = ka[9], k10 = ka[10];
  asm volatile("" : "+s"(k0), "+s"(k1), "+s"(k2), "+s"(k3), "+s"(k4), "+s"(k5), "+s"(k6), "+s"(k7), "+s"(k8), "+s"(k9), "+s"(k10));
  const gptr x = (gptr)k0, wqkv = (gptr)k1, wo = (gptr)k2, w1 = (gptr)k3, b1 = (gptr)k4, w2 = (gptr)k5, b2 = (gptr)k6, cvec = (gptr)k7, wmod = (gptr)k8;
  GAS unsigned char* const out = (GAS unsigned char*)k9;
  unsigned char* const ws = (unsigned char*)k10;
  LAS unsigned char* L = (LAS unsigned char*)smem;
  const int tid = threadIdx.x, lane = tid & 63, b = blockIdx.x;
  const int w = __builtin_amdgcn_readfirstlane(tid >> 6);
  const int xcd = b & 7, jj = b >> 3;
  const int p = (xcd >> 1) * 8 + (jj >> 2), g = (xcd & 1) * 4 + (jj & 3);
  const rsrc_t rs = __builtin_amdgcn_make_buffer_rsrc(ws, 0, WS_USED, 0x00020000);
  const int g4 = lane >> 4, t16 = lane & 15;
  const u32 l16 = lane * 16;
  STAMP(0);

  const u64 Tv = __hip_atomic_load((const GAS u64*)(ws + OFF_E), __ATOMIC_RELAXED, __HIP_MEMORY_SCOPE_AGENT) + 1;
  const int job = w * 256 + b;
  const bool hasjob = job < 1612;
  gptr jsrc; u32 jstride, jdst, jA, jB; int ppr; bool paired;
  if (job < 480) {
    const int gg = job / 60, rem = job % 60, part = rem / 20, j = rem % 20;
    jsrc = wqkv + (u32)(32 * j * 3840 + part * 1280 + gg * 160);
    jstride = 3840; ppr = 10; paired = false;
    jdst = OFF_WQ + (gg * 15 + part * 5) * 20480 + j * 1024; jA = 40960; jB = 20480;
  } else if (job < 640) {
    const int id = job - 480, gg = id / 20, j = id % 20;
    jsrc = wo + (u32)(32 * j * 1280 + gg * 160);
    jstride = 1280; ppr = 10; paired = true;
    jdst = OFF_WO + (gg * 20 + j) * 5120; jA = 2048; jB = 1024;
  } else if (job < 1072) {
    const int id = job - 640, gg = id / 54, j = id % 54;
    jsrc = w2 + (u32)(32 * j * 1280 + gg * 160);
    jstride = 1280; ppr = 10; paired = true;
    jdst = OFF_W2 + (gg * 54 + j) * 5120; jA = 2048; jB = 1024;
  } else {
    const int id = hasjob ? job - 1072 : 0, jb2 = id / 20, j = id % 20;
    jsrc = w1 + (u32)(32 * j * 3456 + jb2 * 128);
    jstride = 3456; ppr = 8; paired = true;
    jdst = OFF_W1 + (2 * jb2 * 20 + j) * 2048; jA = 40960; jB = 1024;
  }
  const int cl = b % 60, ks = b / 60;
  v4u gw[3]; u16 gc[3];
  if (b < 240) {
#pragma unroll
    for (int i = 0; i < 3; ++i) {
      const int rr = (lane >> 3) + 8 * i;
      const u32 k = 160 * ks + 20 * w + (rr < 20 ? rr : 0);
      gw[i] = *(const GAS v4u*)(wmod + (k * 7680 + cl * 128 + (lane & 7) * 16));
      gc[i] = *(const GAS u16*)(cvec + k * 2);
    }
  }
  v4u jr[5];
  if (hasjob) {
    const u32 magic = ppr == 10 ? 6554u : 8192u;
#pragma unroll
    for (int it = 0; it < 5; ++it) {
      u32 idx = it * 64 + lane;
      if (it == 4 && ppr == 8) idx = lane;
      const u32 row = (idx * magic) >> 16, pc = idx - row * ppr;
      jr[it] = *(const GAS v4u*)(jsrc + (row * jstride + pc * 16));
    }
  }
  v4u xr[3][5];
#pragma unroll
  for (int a = 0; a < 3; ++a) {
    if (a < 2 || w < 4) {
      int tok = 64 * p - 16 + 4 * (w + 8 * a) + g4;
      tok = tok < 0 ? 0 : tok;
#pragma unroll
      for (int m = 0; m < 5; ++m) xr[a][m] = *(const GAS v4u*)(x + (u32)(tok * 1280 + (t16 + 16 * m) * 16));
    } else {
#pragma unroll
      for (int m = 0; m < 5; ++m) xr[a][m] = v4u{0, 0, 0, 0};
    }
  }
  u32 pf = 0;
  if (w == 7) {
    u64 pc; int len;
    asm volatile("fused_pf_begin:\n\ts_getpc_b64 %0\n\ts_mov_b32 %1, fused_pf_end-fused_pf_begin" : "=s"(pc), "=s"(len));
    const int o = (jj * 16 + t16) * 128;
    if (o < len) pf = *(const GAS u32*)(pc + (u32)o);
  }
  SCHED();
  STAMP(18);
  if (b < 240) {
    float acc[8];
#pragma unroll
    for (int e = 0; e < 8; ++e) acc[e] = 0.f;
#pragma unroll
    for (int i = 0; i < 3; ++i) {
      const float c = ((lane >> 3) + 8 * i < 20) ? bf2f(gc[i]) : 0.f;
      const float s = c * __builtin_amdgcn_rcpf(1.f + __expf(-c));
#pragma unroll
      for (int e = 0; e < 4; ++e) { acc[2 * e] += s * blo(gw[i][e]); acc[2 * e + 1] += s * bhi(gw[i][e]); }
    }
#pragma unroll
    for (int e = 0; e < 8; ++e) acc[e] = x32_sum(x16_sum(dpp_add<0x128>(acc[e])));
    if (lane < 8) {
      *(LAS f32x4*)(L + L_GV + w * 256 + lane * 32) = f32x4{acc[0], acc[1], acc[2], acc[3]};
      *(LAS f32x4*)(L + L_GV + w * 256 + lane * 32 + 16) = f32x4{acc[4], acc[5], acc[6], acc[7]};
    }
  }
  STAMP(19);
  if (hasjob) {
    LAS unsigned char* sc = L + w * 5120;
#pragma unroll
    for (int it = 0; it < 5; ++it)
      if (it < 4 || ppr == 10) *(LAS v4u*)(sc + it * 1024 + l16) = jr[it];
    __builtin_amdgcn_fence(__ATOMIC_RELEASE, "wavefront");
    __builtin_amdgcn_wave_barrier();
    __builtin_amdgcn_fence(__ATOMIC_ACQUIRE, "wavefront");
    const int rowb = ppr * 16;
    const int q = t16 >> 2, pp = lane & 3;
    const int rbase = (8 * g4 + q) * rowb;
#pragma unroll
    for (int ct = 0; ct < 5; ++ct) {
      if (ct < 4 || ppr == 10) {
        const int cb = (paired && ct < 4) ? 64 * (ct >> 1) + 8 * (ct & 1) + 16 * pp : 32 * ct + 8 * pp;
        const v2u lo = ldstr(sc, rbase + cb), hi = ldstr(sc, rbase + 4 * rowb + cb);
        stw(rs, v4u{lo.x, lo.y, hi.x, hi.y}, l16, jdst + (ct >> 1) * jA + (ct & 1) * jB);
      }
    }
  }
  bar();
  if (b < 240 && w == 0) {
    float s = 0.f;
#pragma unroll
    for (int ww = 0; ww < 8; ++ww) s += *(LAS float*)(L + L_GV + ww * 256 + lane * 4);
    __builtin_amdgcn_raw_buffer_store_b32(__builtin_bit_cast(u32, s), rs, lane * 4, OFF_MODP + (ks * 3840 + cl * 64) * 4, SC1);
  }
  STAMP(20);
  drain();
  asm volatile("" ::"v"(pf));
  bar();
  const u32 Tlo = __builtin_amdgcn_readfirstlane((u32)Tv), Thi = __builtin_amdgcn_readfirstlane((u32)(Tv >> 32));
  const u64 T = ((u64)Thi << 32) | Tlo;
  if (tid == 0) publish(ws, OFF_F0 + b * 128, T);
  STAMP(1);
#pragma unroll
  for (int a = 0; a < 3; ++a) {
    if (a < 2 || w < 4) {
      float rstd, nb;
      row_stats(xr[a], rstd, nb);
      const f32x2 r2 = {rstd, rstd}, n2 = {nb, nb};
#pragma unroll
      for (int m = 0; m < 5; ++m) {
#pragma unroll
        for (int e = 0; e < 4; ++e) xr[a][m][e] = pk(fma2(unpk(xr[a][m][e]), r2, n2));
        asm volatile("" : "+v"(xr[a][m]));
      }
    }
  }
  STAMP(2);
  if (w < 4) poll(ws, OFF_F0 + w * 64 * 128, 64, lane, T);
  bar();
  STAMP(3);
  if (b == 0 && tid == 0) publish(ws, OFF_E, T);

  v4u pv1[4];
  if (tid < 320) {
#pragma unroll
    for (int k = 0; k < 4; ++k) pv1[k] = ldw(rs, tid * 16, OFF_MODP + k * 15360);
  }
  constexpr int R1 = 8;
  v4u wr[R1][2];
  const u32 wq0 = OFF_WQ + (g * 15 + 2 * w) * 20480, wq1 = OFF_WQ + (g * 15 + (w < 7 ? 2 * w + 1 : 14)) * 20480;
#pragma unroll
  for (int j = 0; j < R1; ++j) { wr[j][0] = ldw(rs, l16 + (j & 3) * 1024, wq0 + (j >> 2) * 4096); wr[j][1] = ldw(rs, l16 + (j & 3) * 1024, wq1 + (j >> 2) * 4096); }
  SCHED();
  if (tid < 320) {
    f32x4 s = {0.f, 0.f, 0.f, 0.f};
#pragma unroll
    for (int k = 0; k < 4; ++k) s += __builtin_bit_cast(f32x4, pv1[k]);
    if (tid >= 160) s += 1.f;
    *(LAS f32x4*)(L + L_VEC + tid * 16) = s;
  }
  bar();
  f32x4 acc1[5][2];
#pragma unroll
  for (int i = 0; i < 5; ++i) { acc1[i][0] = f32x4{0.f, 0.f, 0.f, 0.f}; acc1[i][1] = f32x4{0.f, 0.f, 0.f, 0.f}; }
  const int rd_even = g4 * 256 + ((t16 ^ g4) * 16), rd_odd = rd_even ^ 64;
  auto prep1 = [&](int m, int a) {
    const int k0 = (t16 + 16 * m) * 8;
    const f32x4 s0 = ldsf4(L, L_VEC + k0 * 4), s1 = ldsf4(L, L_VEC + k0 * 4 + 16);
    const f32x4 c0 = ldsf4(L, L_VEC + 2560 + k0 * 4), c1 = ldsf4(L, L_VEC + 2560 + k0 * 4 + 16);
    const int grp = w + 8 * a, tt = grp >> 2, t = 4 * (grp & 3) + g4;
    const v4u r = xr[a][m];
    v4u o;
    o.x = pk(fma2(unpk(r.x), c0.xy, s0.xy));
    o.y = pk(fma2(unpk(r.y), c0.zw, s0.zw));
    o.z = pk(fma2(unpk(r.z), c1.xy, s1.xy));
    o.w = pk(fma2(unpk(r.w), c1.zw, s1.zw));
    *(LAS v4u*)(L + L_A + tt * 20480 + (4 * m + (t16 >> 2)) * 1024 + (t16 & 3) * 256 + ((t ^ (t16 & 7)) * 16)) = o;
  };
  prep1(0, 0); prep1(0, 1);
  if (w < 4) prep1(0, 2);
  bar();
  STAMP(4);
  {
    v4u af[2][5];
#pragma unroll
    for (int tt = 0; tt < 5; ++tt) af[0][tt] = lds128(L, L_A + tt * 20480 + rd_even);
#pragma unroll
    for (int j = 0; j < 20; ++j) {
      const int m = j >> 2, q = j & 3;
      if (j + 1 < 20) {
#pragma unroll
        for (int tt = 0; tt < 5; ++tt) af[(j + 1) & 1][tt] = lds128(L, L_A + tt * 20480 + (j + 1) * 1024 + (((j + 1) & 1) ? rd_odd : rd_even));
      }
      const v4u wa = wr[j % R1][0], wb = wr[j % R1][1];
      if (j + R1 < 20) {
        wr[j % R1][0] = ldw(rs, l16 + ((j + R1) & 3) * 1024, wq0 + ((j + R1) >> 2) * 4096);
        wr[j % R1][1] = ldw(rs, l16 + ((j + R1) & 3) * 1024, wq1 + ((j + R1) >> 2) * 4096);
      }
      if (m < 4 && q < 2) prep1(m + 1, q);
#pragma unroll
      for (int tt = 0; tt < 5; ++tt) {
        acc1[tt][0] = mfma(wa, af[j & 1][tt], acc1[tt][0]);
        acc1[tt][1] = mfma(wb, af[j & 1][tt], acc1[tt][1]);
      }
      SGB(0x100, 9); SGB(0x020, 2);
#pragma unroll
      for (int i = 0; i < 10; ++i) { SGB(0x008, 1); SGB(0x002, 3); }
      SCHED();
      if (m < 4 && q == 2) {
        if (w < 4) prep1(m + 1, 2);
        bar();
      }
    }
  }
  bar();
  STAMP(5);
#pragma unroll
  for (int tt = 0; tt < 5; ++tt)
#pragma unroll
    for (int c = 0; c < 2; ++c)
      if (c == 0 || w < 7)
        *(LAS v2u*)(L + L_A + (tt * 16 + t16) * QS + (2 * w + c) * 32 + g4 * 8) =
            v2u{pack2(acc1[tt][c].x, acc1[tt][c].y), pack2(acc1[tt][c].z, acc1[tt][c].w)};
  bar();
  STAMP(6);
  {
    const int h = w & 3, par = w >> 2;
    const float cs = 0.22360679774997896f * 1.4426950408889634f;
#pragma unroll
    for (int qt = 0; qt < 2; ++qt) {
      const int qrow = 2 * (8 + 16 * qt + t16) + par;
      const int qo = L_A + qrow * QS + 40 * h + (g4 < 3 ? 16 * g4 : 0);
      v2u qlo = lds64(L, qo), qhi = lds64(L, qo + 8);
      if (g4 == 3) qlo = v2u{0, 0};
      if (g4 >= 2) qhi = v2u{0, 0};
      const v4u qf = {qlo.x, qlo.y, qhi.x, qhi.y};
      f32x4 sc[2];
#pragma unroll
      for (int kt = 0; kt < 2; ++kt) {
        int ki = 16 * qt + 16 * kt + t16;
        ki = ki > 39 ? 39 : ki;
        const int ko = L_A + (2 * ki + par) * QS + 160 + 40 * h + 16 * g4;
        const v2u klo = lds64(L, ko), khi = lds64(L, ko + 8);
        sc[kt] = mfma(v4u{klo.x, klo.y, khi.x, khi.y}, qf, f32x4{0.f, 0.f, 0.f, 0.f});
      }
      const int vq = t16 >> 2, vp = lane & 3;
      int ka2 = 16 * qt + 4 * g4 + vq, kb = ka2 + 16;
      kb = kb > 39 ? 39 : kb;
      const int va = L_A + (2 * ka2 + par) * QS + 320 + 40 * h, vb = L_A + (2 * kb + par) * QS + 320 + 40 * h;
      const v2u a0 = ldstr(L, va + 8 * vp), b0 = ldstr(L, vb + 8 * vp);
      const v2u a1 = ldstr(L, va + 32), b1v = ldstr(L, vb + 32);
      float mx = -3.0e38f;
#pragma unroll
      for (int kt = 0; kt < 2; ++kt)
#pragma unroll
        for (int r = 0; r < 4; ++r) {
          const int kr = 16 * kt + 4 * g4 + r;
          bool ok = (unsigned)(kr - t16 - 1) < 8u;
          if (qt == 0) ok = ok && (p > 0 || kr >= 8);
          sc[kt][r] = ok ? sc[kt][r] : -3.0e38f;
          mx = fmaxf(mx, sc[kt][r]);
        }
      mx = x32_max(x16_max(mx));
      float sum = 0.f;
#pragma unroll
      for (int kt = 0; kt < 2; ++kt)
#pragma unroll
        for (int r = 0; r < 4; ++r) { sc[kt][r] = __builtin_amdgcn_exp2f((sc[kt][r] - mx) * cs); sum += sc[kt][r]; }
      sum = x32_sum(x16_sum(sum));
      const float inv = __builtin_amdgcn_rcpf(sum);
      const v4u pf4 = {pack2(sc[0].x * inv, sc[0].y * inv), pack2(sc[0].z * inv, sc[0].w * inv), pack2(sc[1].x * inv, sc[1].y * inv), pack2(sc[1].z * inv, sc[1].w * inv)};
      const f32x4 o0 = mfma(v4u{a0.x, a0.y, b0.x, b0.y}, pf4, f32x4{0.f, 0.f, 0.f, 0.f});
      const f32x4 o1 = mfma(v4u{a1.x, a1.y, b1v.x, b1v.y}, pf4, f32x4{0.f, 0.f, 0.f, 0.f});
      const int orow = L_OB + (par * 32 + 16 * qt + t16) * OS + 40 * h;
      *(LAS v2u*)(L + orow + 8 * g4) = v2u{pack2(o0.x, o0.y), pack2(o0.z, o0.w)};
      if (g4 == 0) *(LAS v2u*)(L + orow + 32) = v2u{pack2(o1.x, o1.y), pack2(o1.z, o1.w)};
    }
  }
  bar();
  STAMP(21);
  {
    const int tok = 2 * (lane & 31) + (lane >> 5);
#pragma unroll
    for (int i = 0; i < 2; ++i) {
      const int u = w + 8 * i;
      if (u < 10) {
        const int ch = 10 * g + u;
        const v4u v = lds128(L, L_OB + lane * OS + 16 * u);
        stw(rs, v, (tok >> 4) * 20480 + (tok & 15) * 16, OFF_O + p * 81920 + (ch >> 2) * 1024 + (ch & 3) * 256);
      }
    }
  }
  drain();
  bar();
  if (tid == 0) publish(ws, OFF_F1 + (p * 8 + g) * 128, T);
  STAMP(7);

  const int s4 = w & 3, kh = w >> 2;
  const int tokg = 64 * p + 16 * s4 + t16;
  const int fo = kh == 0 ? 8 * g4 : 32 + 8 * g4;
  const v4u xa = *(const GAS v4u*)(x + (u32)(tokg * 1280 + (80 * g + fo) * 2));
  const v2u xb = *(const GAS v2u*)(x + (u32)(tokg * 1280 + (80 * g + 64 + 4 * g4) * 2));
  if (tid < 320) {
    f32x4 s = {0.f, 0.f, 0.f, 0.f};
    v4u pv[4];
#pragma unroll
    for (int k = 0; k < 4; ++k) pv[k] = ldw(rs, tid * 16, OFF_MODP + k * 15360 + 1920 * 4);
#pragma unroll
    for (int k = 0; k < 4; ++k) s += __builtin_bit_cast(f32x4, pv[k]);
    if (tid >= 160) s += 1.f;
    *(LAS f32x4*)(L + L_VEC + tid * 16) = s;
  } else if (tid < 360) {
    const int i = tid - 320, which = i / 20, c4 = i % 20;
    f32x4 s = {0.f, 0.f, 0.f, 0.f};
    v4u pv[4];
#pragma unroll
    for (int k = 0; k < 4; ++k) pv[k] = ldw(rs, (which ? 3200 : 1280) * 4 + c4 * 16, OFF_MODP + k * 15360 + g * 320);
#pragma unroll
    for (int k = 0; k < 4; ++k) s += __builtin_bit_cast(f32x4, pv[k]);
    *(LAS f32x4*)(L + L_SM + which * 320 + c4 * 16) = s;
  } else if (tid >= 384 && tid < 404) {
    const int c4 = tid - 384;
    const v2u v = *(const GAS v2u*)(b2 + (80 * g + 4 * c4) * 2);
    *(LAS f32x4*)(L + L_SM + 640 + c4 * 16) = f32x4{blo(v.x), bhi(v.x), blo(v.y), bhi(v.y)};
  }
  if (w < 7) {
#pragma unroll
    for (int i = 0; i < 4; ++i) {
      const int gi = w + 7 * i;
      if (gi < 25) { dma<4>(rs, l16, OFF_WO + g * 102400 + gi * 4096, L_A + gi * 4096); VMCNT(4); }
    }
    VMCNT(0);
  } else {
    poll(ws, OFF_F1 + p * 8 * 128, 8, lane, T);
  }
  bar();
  STAMP(8);
  v4u oa[10];
#pragma unroll
  for (int i = 0; i < 10; ++i) oa[i] = ldw(rs, l16 + (i & 3) * 1024, OFF_O + (p * 4 + s4) * 20480 + kh * 10240 + (i >> 2) * 4096);
  SCHED();
  f32x4 acc2[5];
#pragma unroll
  for (int i = 0; i < 5; ++i) acc2[i] = f32x4{0.f, 0.f, 0.f, 0.f};
  {
    v4u wf[2][5];
#pragma unroll
    for (int ct = 0; ct < 5; ++ct) wf[0][ct] = lds128(L, L_A + ((10 * kh) * 5 + ct) * 1024 + l16);
#pragma unroll
    for (int i = 0; i < 10; ++i) {
      if (i + 1 < 10) {
#pragma unroll
        for (int ct = 0; ct < 5; ++ct) wf[(i + 1) & 1][ct] = lds128(L, L_A + ((10 * kh + i + 1) * 5 + ct) * 1024 + l16);
      }
      SCHED();
#pragma unroll
      for (int ct = 0; ct < 5; ++ct) acc2[ct] = mfma(wf[i & 1][ct], oa[i], acc2[ct]);
      SCHED();
    }
  }
  if (kh == 0) {
    *(LAS f32x4*)(L + L_RED + (s4 * 5 + 2) * 1024 + l16) = acc2[2];
    *(LAS f32x4*)(L + L_RED + (s4 * 5 + 3) * 1024 + l16) = acc2[3];
  } else {
    *(LAS f32x4*)(L + L_RED + (s4 * 5 + 0) * 1024 + l16) = acc2[0];
    *(LAS f32x4*)(L + L_RED + (s4 * 5 + 1) * 1024 + l16) = acc2[1];
    *(LAS f32x4*)(L + L_RED + (s4 * 5 + 4) * 1024 + l16) = acc2[4];
  }
  bar();
  STAMP(9);
  v4u x1a; v2u x1b = {0, 0};
  {
    const int c0 = kh == 0 ? 0 : 2;
    const f32x4 u0 = (kh == 0 ? acc2[0] : acc2[2]) + ldsf4(L, L_RED + (s4 * 5 + c0) * 1024 + l16);
    const f32x4 u1 = (kh == 0 ? acc2[1] : acc2[3]) + ldsf4(L, L_RED + (s4 * 5 + c0 + 1) * 1024 + l16);
    const f32x4 g0 = ldsf4(L, L_SM + fo * 4), g1 = ldsf4(L, L_SM + fo * 4 + 16);
    x1a.x = pk(fma2(g0.xy, u0.xy, unpk(xa.x)));
    x1a.y = pk(fma2(g0.zw, u0.zw, unpk(xa.y)));
    x1a.z = pk(fma2(g1.xy, u1.xy, unpk(xa.z)));
    x1a.w = pk(fma2(g1.zw, u1.zw, unpk(xa.w)));
    stw(rs, x1a, tokg * 1280 + (80 * g + fo) * 2, OFF_X1);
    if (kh == 0) {
      const f32x4 u2 = acc2[4] + ldsf4(L, L_RED + (s4 * 5 + 4) * 1024 + l16);
      const f32x4 g2 = ldsf4(L, L_SM + (64 + 4 * g4) * 4);
      x1b.x = pk(fma2(g2.xy, u2.xy, unpk(xb.x)));
      x1b.y = pk(fma2(g2.zw, u2.zw, unpk(xb.y)));
      __builtin_amdgcn_raw_buffer_store_b64(x1b, rs, tokg * 1280 + (80 * g + 64 + 4 * g4) * 2, OFF_X1, SC1);
    }
  }
  drain();
  bar();
  if (tid == 0) publish(ws, OFF_F2 + (p * 8 + g) * 128, T);
  STAMP(10);

  const int j0 = g < 7 ? 7 * g : 48, nk = g < 6 ? 7 : 6;
  const bool act3 = w < nk;
  const int jb = j0 + (act3 ? w : 0);
  constexpr int R3 = 8;
  v4u w3[R3][2];
  const u32 w1o = OFF_W1 + jb * 40960;
  v4u b1v = {0, 0, 0, 0};
  if (act3) {
#pragma unroll
    for (int j = 0; j < 4; ++j) { w3[j][0] = ldw(rs, l16 + (j & 1) * 2048, w1o + (j >> 1) * 4096); w3[j][1] = ldw(rs, l16 + (j & 1) * 2048 + 1024, w1o + (j >> 1) * 4096); }
    b1v = *(const GAS v4u*)(b1 + (32 * jb + 8 * g4) * 2);
  }
  SCHED();
  if (w == 7) poll(ws, OFF_F2 + p * 8 * 128, 8, lane, T);
  bar();
  STAMP(11);
  v4u yr[2][5];
#pragma unroll
  for (int a = 0; a < 2; ++a)
#pragma unroll
    for (int m = 0; m < 5; ++m) yr[a][m] = ldw(rs, (64 * p + 8 * w + 4 * a + g4) * 1280 + (t16 + 16 * m) * 16, OFF_X1);
  if (act3) {
#pragma unroll
    for (int j = 4; j < R3; ++j) { w3[j][0] = ldw(rs, l16 + (j & 1) * 2048, w1o + (j >> 1) * 4096); w3[j][1] = ldw(rs, l16 + (j & 1) * 2048 + 1024, w1o + (j >> 1) * 4096); }
  }
  SCHED();
#pragma unroll
  for (int a = 0; a < 2; ++a) {
    asm volatile("" ::: "memory");
    SCHED();
    float rstd, nb;
    row_stats(yr[a], rstd, nb);
    const f32x2 r2 = {rstd, rstd}, n2 = {nb, nb};
#pragma unroll
    for (int m = 0; m < 5; ++m) {
      const int k0 = (t16 + 16 * m) * 8;
      const f32x4 s0 = ldsf4(L, L_VEC + k0 * 4), s1 = ldsf4(L, L_VEC + k0 * 4 + 16);
      const f32x4 c0 = ldsf4(L, L_VEC + 2560 + k0 * 4), c1 = ldsf4(L, L_VEC + 2560 + k0 * 4 + 16);
      yr[a][m].x = pk(fma2(fma2(unpk(yr[a][m].x), r2, n2), c0.xy, s0.xy));
      yr[a][m].y = pk(fma2(fma2(unpk(yr[a][m].y), r2, n2), c0.zw, s0.zw));
      yr[a][m].z = pk(fma2(fma2(unpk(yr[a][m].z), r2, n2), c1.xy, s1.xy));
      yr[a][m].w = pk(fma2(fma2(unpk(yr[a][m].w), r2, n2), c1.zw, s1.zw));
    }
    row_stats(yr[a], rstd, nb);
    const f32x2 r3 = {rstd, rstd}, n3 = {nb, nb};
    const int row = 8 * w + 4 * a + g4, tt = row >> 4, t = row & 15;
#pragma unroll
    for (int m = 0; m < 5; ++m) {
      const v4u o = {pk(fma2(unpk(yr[a][m].x), r3, n3)), pk(fma2(unpk(yr[a][m].y), r3, n3)),
                     pk(fma2(unpk(yr[a][m].z), r3, n3)), pk(fma2(unpk(yr[a][m].w), r3, n3))};
      *(LAS v4u*)(L + L_A + tt * 20480 + (4 * m + (t16 >> 2)) * 1024 + (t16 & 3) * 256 + ((t ^ (t16 & 7)) * 16)) = o;
    }
  }
  bar();
  STAMP(12);
  const u32 w2g = OFF_W2 + g * 54 * 5120;
  f32x4 acc3[4][2];
#pragma unroll
  for (int i = 0; i < 4; ++i) { acc3[i][0] = f32x4{0.f, 0.f, 0.f, 0.f}; acc3[i][1] = f32x4{0.f, 0.f, 0.f, 0.f}; }
  if (act3) {
    v4u af[2][4];
#pragma unroll
    for (int tt = 0; tt < 4; ++tt) af[0][tt] = lds128(L, L_A + tt * 20480 + rd_even);
#pragma unroll
    for (int j = 0; j < 20; ++j) {
      if (j + 1 < 20) {
#pragma unroll
        for (int tt = 0; tt < 4; ++tt) af[(j + 1) & 1][tt] = lds128(L, L_A + tt * 20480 + (j + 1) * 1024 + (((j + 1) & 1) ? rd_odd : rd_even));
      }
      const v4u wa = w3[j % R3][0], wb = w3[j % R3][1];
      if (j + R3 < 20) {
        w3[j % R3][0] = ldw(rs, l16 + ((j + R3) & 1) * 2048, w1o + ((j + R3) >> 1) * 4096);
        w3[j % R3][1] = ldw(rs, l16 + ((j + R3) & 1) * 2048 + 1024, w1o + ((j + R3) >> 1) * 4096);
      }
      SCHED();
#pragma unroll
      for (int tt = 0; tt < 4; ++tt) {
        acc3[tt][0] = mfma(wa, af[j & 1][tt], acc3[tt][0]);
        acc3[tt][1] = mfma(wb, af[j & 1][tt], acc3[tt][1]);
      }
      SCHED();
    }
  } else if (w == 7) {
#pragma unroll 1
    for (int i = 0; i < 17; ++i) { dma<4>(rs, l16, w2g + (27 * 5 + 5 + 4 * i) * 1024, 81920 + i * 4096); VMCNT(8); }
    dma<2>(rs, l16, w2g + (27 * 5 + 5 + 68) * 1024, 81920 + 68 * 1024);
  }
  bar();
  STAMP(13);
  if (act3) {
    const float bb[8] = {blo(b1v.x), bhi(b1v.x), blo(b1v.y), bhi(b1v.y), blo(b1v.z), bhi(b1v.z), blo(b1v.w), bhi(b1v.w)};
    const float k1c = -2.f * 0.7978845608028654f * 1.4426950408889634f, k3c = k1c * 0.044715f;
#pragma unroll
    for (int tt = 0; tt < 4; ++tt) {
      float hv[8];
#pragma unroll
      for (int e = 0; e < 8; ++e) {
        const float v = acc3[tt][e >> 2][e & 3] + bb[e];
        const float ex = __builtin_amdgcn_exp2f(v * (k1c + k3c * v * v));
        hv[e] = v * __builtin_amdgcn_rcpf(1.f + ex);
      }
      stw(rs, v4u{pack2(hv[0], hv[1]), pack2(hv[2], hv[3]), pack2(hv[4], hv[5]), pack2(hv[6], hv[7])}, l16, OFF_HH + ((p * 4 + tt) * 54 + jb) * 1024);
    }
  } else if (w == 7) {
#pragma unroll 1
    for (int i = 0; i < 18; ++i) dma<4>(rs, l16, w2g + 4 * i * 1024, i * 4096);
    dma<3>(rs, l16, w2g + 72 * 1024, 72 * 1024);
    dma<4>(rs, l16, w2g + 27 * 5 * 1024, 76800);
    dma<1>(rs, l16, w2g + (27 * 5 + 4) * 1024, 76800 + 4096);
  }
  if (act3) drain();
  bar();
  if (tid == 0) publish(ws, OFF_F3 + (p * 8 + g) * 128, T);
  STAMP(14);

  const u32 w2o = w2g + 27 * kh * 5120;
  const u32 ring = kh * 76800;
  if (w == 7) poll(ws, OFF_F3 + p * 8 * 128, 8, lane, T);
  bar();
  STAMP(15);
  v4u ha[27];
#pragma unroll
  for (int i = 0; i < 27; ++i) ha[i] = ldw(rs, l16 + (i & 3) * 1024, OFF_HH + ((p * 4 + s4) * 54 + 27 * kh) * 1024 + (i >> 2) * 4096);
  SCHED();
  f32x4 acc4[5];
#pragma unroll
  for (int i = 0; i < 5; ++i) acc4[i] = f32x4{0.f, 0.f, 0.f, 0.f};
  {
    v4u wf[2][5];
#pragma unroll
    for (int ct = 0; ct < 5; ++ct) wf[0][ct] = lds128(L, ring + ct * 1024 + l16);
#pragma unroll
    for (int i = 0; i < 27; ++i) {
      const int n = i / 3;
      if (i + 1 < 27) {
#pragma unroll
        for (int ct = 0; ct < 5; ++ct) wf[(i + 1) & 1][ct] = lds128(L, ring + (((i + 1) % 15) * 5 + ct) * 1024 + l16);
      }
      SCHED();
#pragma unroll
      for (int ct = 0; ct < 5; ++ct) acc4[ct] = mfma(wf[i & 1][ct], ha[i], acc4[ct]);
      SCHED();
      if (i % 3 == 2) {
        if (n == 3 || n == 4) VMCNT(6);
        if (n == 5) VMCNT(3);
        if (n == 6) VMCNT(0);
        bar();
        if (n < 4) {
          if (s4 < 3) dma<4>(rs, l16, w2o + (75 + 15 * n + 4 * s4) * 1024, ring + (15 * n + 4 * s4) * 1024);
          else dma<3>(rs, l16, w2o + (75 + 15 * n + 12) * 1024, ring + (15 * n + 12) * 1024);
        }
      }
    }
  }
  STAMP(16);
  if (kh == 0) {
    *(LAS f32x4*)(L + L_RED + (s4 * 5 + 2) * 1024 + l16) = acc4[2];
    *(LAS f32x4*)(L + L_RED + (s4 * 5 + 3) * 1024 + l16) = acc4[3];
  } else {
    *(LAS f32x4*)(L + L_RED + (s4 * 5 + 0) * 1024 + l16) = acc4[0];
    *(LAS f32x4*)(L + L_RED + (s4 * 5 + 1) * 1024 + l16) = acc4[1];
    *(LAS f32x4*)(L + L_RED + (s4 * 5 + 4) * 1024 + l16) = acc4[4];
  }
  bar();
  {
    const int c0 = kh == 0 ? 0 : 2;
    const f32x4 u0 = (kh == 0 ? acc4[0] : acc4[2]) + ldsf4(L, L_RED + (s4 * 5 + c0) * 1024 + l16);
    const f32x4 u1 = (kh == 0 ? acc4[1] : acc4[3]) + ldsf4(L, L_RED + (s4 * 5 + c0 + 1) * 1024 + l16);
    const f32x4 g0 = ldsf4(L, L_SM + 320 + fo * 4), g1 = ldsf4(L, L_SM + 320 + fo * 4 + 16);
    const f32x4 e0 = ldsf4(L, L_SM + 640 + fo * 4), e1 = ldsf4(L, L_SM + 640 + fo * 4 + 16);
    v4u o;
    o.x = pk(fma2(g0.xy, u0.xy + e0.xy, unpk(x1a.x)));
    o.y = pk(fma2(g0.zw, u0.zw + e0.zw, unpk(x1a.y)));
    o.z = pk(fma2(g1.xy, u1.xy + e1.xy, unpk(x1a.z)));
    o.w = pk(fma2(g1.zw, u1.zw + e1.zw, unpk(x1a.w)));
    *(GAS v4u*)(out + (u32)(tokg * 1280 + (80 * g + fo) * 2)) = o;
    if (kh == 0) {
      const f32x4 u2 = acc4[4] + ldsf4(L, L_RED + (s4 * 5 + 4) * 1024 + l16);
      const f32x4 g2 = ldsf4(L, L_SM + 320 + (64 + 4 * g4) * 4), e2 = ldsf4(L, L_SM + 640 + (64 + 4 * g4) * 4);
      const v2u o2 = {pk(fma2(g2.xy, u2.xy + e2.xy, unpk(x1b.x))), pk(fma2(g2.zw, u2.zw + e2.zw, unpk(x1b.y)))};
      *(GAS v2u*)(out + (u32)(tokg * 1280 + (80 * g + 64 + 4 * g4) * 2)) = o2;
    }
  }
  STAMP(17);
  asm volatile("fused_pf_end:" ::: "memory");
}

extern "C" void kernel_launch(void* const* d_in, const int* in_sizes, int n_in, void* d_out, int out_size, void* d_ws, size_t ws_size,
                              hipStream_t stream) {
  (void)in_sizes; (void)n_in; (void)out_size; (void)ws_size;
  (void)hipFuncSetAttribute((const void*)fused_block, hipFuncAttributeMaxDynamicSharedMemorySize, LDS_BYTES);
  fused_block<<<NWG, NTHR, LDS_BYTES, stream>>>((const u16*)d_in[0], (const u16*)d_in[1], (const u16*)d_in[2], (const u16*)d_in[4],
                                                (const u16*)d_in[5], (const u16*)d_in[6], (const u16*)d_in[7], (const u16*)d_in[8],
                                                (const u16*)d_in[9], (u16*)d_out, (unsigned char*)d_ws);
}
```

```cpp
#define PROBE_A 0
#define PROBE_B 17
#include <hip/hip_runtime.h>

typedef unsigned short u16;
typedef unsigned int u32;
typedef unsigned long long u64;
typedef u32 v4u __attribute__((ext_vector_type(4)));
typedef u32 v2u __attribute__((ext_vector_type(2)));
typedef float f32x4 __attribute__((ext_vector_type(4)));
typedef float f32x2 __attribute__((ext_vector_type(2)));
typedef short s16x8 __attribute__((ext_vector_type(8)));
typedef short s16x4 __attribute__((ext_vector_type(4)));
typedef __bf16 bf16x2 __attribute__((ext_vector_type(2)));
#define GAS __attribute__((address_space(1)))
#define LAS __attribute__((address_space(3)))
#define DI __device__ __forceinline__
typedef __amdgpu_buffer_rsrc_t rsrc_t;

#define XN 2

constexpr int NWG = 256, NTHR = 512;
constexpr u32 OFF_E = 0, OFF_F0 = 0x1000, OFF_F1 = 0xA000, OFF_F2 = 0x12000, OFF_F3 = 0x1A000;
constexpr u32 OFF_MODP = 0x30000, OFF_WQ = 0x40000, OFF_WO = 0x300000, OFF_W1 = 0x400000, OFF_W2 = 0x700000;
constexpr u32 OFF_O = 0xA00000, OFF_X1 = 0xD00000, OFF_HH = 0x1000000, WS_USED = 0x1800000;
constexpr u32 OFF_DIAG = 0x1800000;
constexpr int L_A = 0, L_OB = 40960, L_GV = 40960, L_RED = 102400, L_SM = 153600, L_VEC = 154624, LDS_BYTES = 163840;
constexpr int QS = 488;
constexpr int OS = 176;
constexpr int SPIN_CAP = 1 << 14;
constexpr int SC1 = 16;

DI f32x4 mfma(v4u a, v4u b, f32x4 c) {
  return __builtin_amdgcn_mfma_f32_16x16x32_bf16(__builtin_bit_cast(s16x8, a), __builtin_bit_cast(s16x8, b), c, 0, 0, 0);
}
DI u32 pack2(float a, float b) { f32x2 v = {a, b}; return __builtin_bit_cast(u32, __builtin_convertvector(v, bf16x2)); }
DI float blo(u32 u) { return __builtin_bit_cast(float, u << 16); }
DI float bhi(u32 u) { return __builtin_bit_cast(float, u & 0xffff0000u); }
DI f32x2 unpk(u32 u) { return f32x2{blo(u), bhi(u)}; }
DI f32x2 fma2(f32x2 a, f32x2 b, f32x2 c) { return __builtin_elementwise_fma(a, b, c); }
DI u32 pk(f32x2 v) { return __builtin_bit_cast(u32, __builtin_convertvector(v, bf16x2)); }
DI float dot2(u32 a, u32 b, float c) { return __builtin_amdgcn_fdot2_f32_bf16(__builtin_bit_cast(bf16x2, a), __builtin_bit_cast(bf16x2, b), c, false); }
DI float bf2f(u16 v) { return __builtin_bit_cast(float, (u32)v << 16); }
template <int CTRL> DI float dpp_add(float v) {
  return v + __builtin_bit_cast(float, __builtin_amdgcn_update_dpp(0, __builtin_bit_cast(int, v), CTRL, 0xf, 0xf, true));
}
DI float row16_sum(float v) { v = dpp_add<0x128>(v); v = dpp_add<0x124>(v); v = dpp_add<0x122>(v); return dpp_add<0x121>(v); }
DI float x16_sum(float v) {
  auto r = __builtin_amdgcn_permlane16_swap(__builtin_bit_cast(u32, v), __builtin_bit_cast(u32, v), false, false);
  const u32 a = r[0], c = r[1];
  return __builtin_bit_cast(float, a) + __builtin_bit_cast(float, c);
}
DI float x32_sum(float v) {
  auto r = __builtin_amdgcn_permlane32_swap(__builtin_bit_cast(u32, v), __builtin_bit_cast(u32, v), false, false);
  const u32 a = r[0], c = r[1];
  return __builtin_bit_cast(float, a) + __builtin_bit_cast(float, c);
}
DI float x16_max(float v) {
  auto r = __builtin_amdgcn_permlane16_swap(__builtin_bit_cast(u32, v), __builtin_bit_cast(u32, v), false, false);
  const u32 a = r[0], c = r[1];
  return fmaxf(__builtin_bit_cast(float, a), __builtin_bit_cast(float, c));
}
DI float x32_max(float v) {
  auto r = __builtin_amdgcn_permlane32_swap(__builtin_bit_cast(u32, v), __builtin_bit_cast(u32, v), false, false);
  const u32 a = r[0], c = r[1];
  return fmaxf(__builtin_bit_cast(float, a), __builtin_bit_cast(float, c));
}
DI void bar() { asm volatile("s_waitcnt lgkmcnt(0)\n\ts_barrier" ::: "memory"); }
DI void drain() { asm volatile("s_waitcnt vmcnt(0)" ::: "memory"); }
#define SCHED() __builtin_amdgcn_sched_barrier(0)
#define SGB(mask, n) __builtin_amdgcn_sched_group_barrier(mask, n, 0)
#define VMCNT(n) asm volatile("s_waitcnt vmcnt(" #n ")" ::: "memory")
DI v4u ldw(rsrc_t rs, u32 voff, u32 soff) { return __builtin_amdgcn_raw_buffer_load_b128(rs, voff, soff, SC1); }
DI void stw(rsrc_t rs, v4u v, u32 voff, u32 soff) { __builtin_amdgcn_raw_buffer_store_b128(v, rs, voff + soff, 0, SC1); }
DI v4u lds128(LAS unsigned char* L, int off) { return *(LAS v4u*)(L + off); }
DI f32x4 ldsf4(LAS unsigned char* L, int off) { return *(LAS f32x4*)(L + off); }
DI v2u lds64(LAS unsigned char* L, int off) { return *(LAS v2u*)(L + off); }
DI v2u ldstr(LAS unsigned char* L, int off) {
  return __builtin_bit_cast(v2u, __builtin_amdgcn_ds_read_tr16_b64_v4i16((LAS s16x4*)(L + off)));
}
template <int N> DI void dma(rsrc_t rs, u32 voff, u32 soff, u32 ldsoff) {
#define DMA_I(o) "buffer_load_dwordx4 %1, %2, %3 offen offset:" #o " sc1 lds\n\t"
#define DMA_ARGS :: "s"(ldsoff), "v"(voff), "s"(rs), "s"(soff) : "memory"
  if constexpr (N == 4) asm volatile("s_mov_b32 m0, %0\n\ts_nop 0\n\t" DMA_I(0) DMA_I(1024) DMA_I(2048) DMA_I(3072) DMA_ARGS);
  else if constexpr (N == 3) asm volatile("s_mov_b32 m0, %0\n\ts_nop 0\n\t" DMA_I(0) DMA_I(1024) DMA_I(2048) DMA_ARGS);
  else if constexpr (N == 2) asm volatile("s_mov_b32 m0, %0\n\ts_nop 0\n\t" DMA_I(0) DMA_I(1024) DMA_ARGS);
  else asm volatile("s_mov_b32 m0, %0\n\ts_nop 0\n\t" DMA_I(0) DMA_ARGS);
}
DI void poll(unsigned char* ws, u32 off, int n, int lane, u64 T) {
  const GAS u64* f = (const GAS u64*)(ws + off + (lane < n ? lane : 0) * 128);
#pragma unroll 1
  for (int spins = 0; spins < SPIN_CAP; ++spins) {
    const u64 v = __hip_atomic_load(f, __ATOMIC_RELAXED, __HIP_MEMORY_SCOPE_AGENT);
    if (__all(v == T)) break;
    __builtin_amdgcn_s_sleep(1);
  }
  __builtin_amdgcn_fence(__ATOMIC_ACQUIRE, "wavefront");
}
DI void publish(unsigned char* ws, u32 off, u64 T) {
  __hip_atomic_store((GAS u64*)(ws + off), T, __ATOMIC_RELAXED, __HIP_MEMORY_SCOPE_AGENT);
}
DI void row_stats(const v4u (&r)[5], float& rstd, float& nb) {
  float s0 = 0.f, s1 = 0.f, q0 = 0.f, q1 = 0.f;
#pragma unroll
  for (int m = 0; m < 5; ++m) {
    s0 = dot2(r[m].x, 0x3f803f80u, s0); q0 = dot2(r[m].x, r[m].x, q0);
    s1 = dot2(r[m].y, 0x3f803f80u, s1); q1 = dot2(r[m].y, r[m].y, q1);
    s0 = dot2(r[m].z, 0x3f803f80u, s0); q0 = dot2(r[m].z, r[m].z, q0);
    s1 = dot2(r[m].w, 0x3f803f80u, s1); q1 = dot2(r[m].w, r[m].w, q1);
  }
  const float mean = row16_sum(s0 + s1) * (1.f / 640.f);
  const float var = fmaxf(row16_sum(q0 + q1) * (1.f / 640.f) - mean * mean, 0.f);
  rstd = __builtin_amdgcn_rsqf(var + 1e-5f);
  nb = -mean * rstd;
}

#define STAMP(i) do { asm volatile("; MARK " #i ::: "memory"); if ((i) == PROBE_A) probe_a = __builtin_amdgcn_s_memrealtime(); if ((i) == PROBE_B) probe_b = __builtin_amdgcn_s_memrealtime(); } while (0)

typedef const GAS unsigned char* gptr;
__global__ __launch_bounds__(NTHR, 2) void fused_block(const u16* a_x, const u16* a_wqkv, const u16* a_wo, const u16* a_w1, const u16* a_b1,
                                                       const u16* a_w2, const u16* a_b2, const u16* a_c, const u16* a_wmod, u16* a_out,
                                                       unsigned char* a_ws) {
  extern __shared__ __attribute__((aligned(16))) unsigned char smem[];
  u64 probe_k;
  asm volatile("s_memrealtime %0\n\ts_waitcnt lgkmcnt(0)" : "=s"(probe_k) :: "memory");
  const __attribute__((address_space(4))) u64* ka = (const __attribute__((address_space(4))) u64*)__builtin_amdgcn_kernarg_segment_ptr();
  u64 k0 = ka[0], k1 = ka[1], k2 = ka[2], k3 = ka[3], k4 = ka[4], k5 = ka[5], k6 = ka[6], k7 = ka[7], k8 = ka[8], k9 = ka[9], k10 = ka[10];
  asm volatile("" : "+s"(k0), "+s"(k1), "+s"(k2), "+s"(k3), "+s"(k4), "+s"(k5), "+s"(k6), "+s"(k7), "+s"(k8), "+s"(k9), "+s"(k10));
  const gptr x = (gptr)k0, wqkv = (gptr)k1, wo = (gptr)k2, w1 = (gptr)k3, b1 = (gptr)k4, w2 = (gptr)k5, b2 = (gptr)k6, cvec = (gptr)k7, wmod = (gptr)k8;
  GAS unsigned char* const out = (GAS unsigned char*)k9;
  unsigned char* const ws = (unsigned char*)k10;
  LAS unsigned char* L = (LAS unsigned char*)smem;
  const int tid = threadIdx.x, lane = tid & 63, b = blockIdx.x;
  const int w = __builtin_amdgcn_readfirstlane(tid >> 6);
  const int xcd = b & 7, jj = b >> 3;
  const int p = (xcd >> 1) * 8 + (jj >> 2), g = (xcd & 1) * 4 + (jj & 3);
  const rsrc_t rs = __builtin_amdgcn_make_buffer_rsrc(ws, 0, WS_USED, 0x00020000);
  const int g4 = lane >> 4, t16 = lane & 15;
  const u32 l16 = lane * 16;
  u64 probe_a = 0, probe_b = 0;
  STAMP(0);

  const u64 Tv = __hip_atomic_load((const GAS u64*)(ws + OFF_E), __ATOMIC_RELAXED, __HIP_MEMORY_SCOPE_AGENT) + 1;
  const int job = w * 256 + b;
  const bool hasjob = job < 1612;
  gptr jsrc; u32 jstride, jdst, jA, jB; int ppr; bool paired;
  if (job < 480) {
    const int gg = job / 60, rem = job % 60, part = rem / 20, j = rem % 20;
    jsrc = wqkv + (u32)(32 * j * 3840 + part * 1280 + gg * 160);
    jstride = 3840; ppr = 10; paired = false;
    jdst = OFF_WQ + (gg * 15 + part * 5) * 20480 + j * 1024; jA = 40960; jB = 20480;
  } else if (job < 640) {
    const int id = job - 480, gg = id / 20, j = id % 20;
    jsrc = wo + (u32)(32 * j * 1280 + gg * 160);
    jstride = 1280; ppr = 10; paired = true;
    jdst = OFF_WO + (gg * 20 + j) * 5120; jA = 2048; jB = 1024;
  } else if (job < 1072) {
    const int id = job - 640, gg = id / 54, j = id % 54;
    jsrc = w2 + (u32)(32 * j * 1280 + gg * 160);
    jstride = 1280; ppr = 10; paired = true;
    jdst = OFF_W2 + (gg * 54 + j) * 5120; jA = 2048; jB = 1024;
  } else {
    const int id = hasjob ? job - 1072 : 0, jb2 = id / 20, j = id % 20;
    jsrc = w1 + (u32)(32 * j * 3456 + jb2 * 128);
    jstride = 3456; ppr = 8; paired = true;
    jdst = OFF_W1 + (2 * jb2 * 20 + j) * 2048; jA = 40960; jB = 1024;
  }
  const int cl = b % 60, ks = b / 60;
  v4u gw[3]; u16 gc[3];
  if (b < 240) {
#pragma unroll
    for (int i = 0; i < 3; ++i) {
      const int rr = (lane >> 3) + 8 * i;
      const u32 k = 160 * ks + 20 * w + (rr < 20 ? rr : 0);
      gw[i] = *(const GAS v4u*)(wmod + (k * 7680 + cl * 128 + (lane & 7) * 16));
      gc[i] = *(const GAS u16*)(cvec + k * 2);
    }
  }
  v4u jr[5];
  if (hasjob) {
    const u32 magic = ppr == 10 ? 6554u : 8192u;
#pragma unroll
    for (int it = 0; it < 5; ++it) {
      u32 idx = it * 64 + lane;
      if (it == 4 && ppr == 8) idx = lane;
      const u32 row = (idx * magic) >> 16, pc = idx - row * ppr;
      jr[it] = *(const GAS v4u*)(jsrc + (row * jstride + pc * 16));
    }
  }
  v4u xr[3][5];
#pragma unroll
  for (int a = 0; a < 3; ++a) {
    if (a < 2 || w < 4) {
      int tok = 64 * p - 16 + 4 * (w + 8 * a) + g4;
      tok = tok < 0 ? 0 : tok;
#pragma unroll
      for (int m = 0; m < 5; ++m) xr[a][m] = *(const GAS v4u*)(x + (u32)(tok * 1280 + (t16 + 16 * m) * 16));
    } else {
#pragma unroll
      for (int m = 0; m < 5; ++m) xr[a][m] = v4u{0, 0, 0, 0};
    }
  }
  u32 pf = 0;
  if (w == 7) {
    u64 pc; int len;
    asm volatile("fused_pf_begin:\n\ts_getpc_b64 %0\n\ts_mov_b32 %1, fused_pf_end-fused_pf_begin" : "=s"(pc), "=s"(len));
    const int o = (jj * 16 + t16) * 128;
    if (o < len) pf = *(const GAS u32*)(pc + (u32)o);
  }
  SCHED();
  STAMP(18);
  if (b < 240) {
    float acc[8];
#pragma unroll
    for (int e = 0; e < 8; ++e) acc[e] = 0.f;
#pragma unroll
    for (int i = 0; i < 3; ++i) {
      const float c = ((lane >> 3) + 8 * i < 20) ? bf2f(gc[i]) : 0.f;
      const float s = c * __builtin_amdgcn_rcpf(1.f + __expf(-c));
#pragma unroll
      for (int e = 0; e < 4; ++e) { acc[2 * e] += s * blo(gw[i][e]); acc[2 * e + 1] += s * bhi(gw[i][e]); }
    }
#pragma unroll
    for (int e = 0; e < 8; ++e) acc[e] = x32_sum(x16_sum(dpp_add<0x128>(acc[e])));
    if (lane < 8) {
      *(LAS f32x4*)(L + L_GV + w * 256 + lane * 32) = f32x4{acc[0], acc[1], acc[2], acc[3]};
      *(LAS f32x4*)(L + L_GV + w * 256 + lane * 32 + 16) = f32x4{acc[4], acc[5], acc[6], acc[7]};
    }
  }
  STAMP(19);
  if (hasjob) {
    LAS unsigned char* sc = L + w * 5120;
#pragma unroll
    for (int it = 0; it < 5; ++it)
      if (it < 4 || ppr == 10) *(LAS v4u*)(sc + it * 1024 + l16) = jr[it];
    __builtin_amdgcn_fence(__ATOMIC_RELEASE, "wavefront");
    __builtin_amdgcn_wave_barrier();
    __builtin_amdgcn_fence(__ATOMIC_ACQUIRE, "wavefront");
    const int rowb = ppr * 16;
    const int q = t16 >> 2, pp = lane & 3;
    const int rbase = (8 * g4 + q) * rowb;
#pragma unroll
    for (int ct = 0; ct < 5; ++ct) {
      if (ct < 4 || ppr == 10) {
        const int cb = (paired && ct < 4) ? 64 * (ct >> 1) + 8 * (ct & 1) + 16 * pp : 32 * ct + 8 * pp;
        const v2u lo = ldstr(sc, rbase + cb), hi = ldstr(sc, rbase + 4 * rowb + cb);
        stw(rs, v4u{lo.x, lo.y, hi.x, hi.y}, l16, jdst + (ct >> 1) * jA + (ct & 1) * jB);
      }
    }
  }
  bar();
  if (b < 240 && w == 0) {
    float s = 0.f;
#pragma unroll
    for (int ww = 0; ww < 8; ++ww) s += *(LAS float*)(L + L_GV + ww * 256 + lane * 4);
    __builtin_amdgcn_raw_buffer_store_b32(__builtin_bit_cast(u32, s), rs, lane * 4, OFF_MODP + (ks * 3840 + cl * 64) * 4, SC1);
  }
  STAMP(20);
  drain();
  asm volatile("" ::"v"(pf));
  bar();
  const u32 Tlo = __builtin_amdgcn_readfirstlane((u32)Tv), Thi = __builtin_amdgcn_readfirstlane((u32)(Tv >> 32));
  const u64 T = ((u64)Thi << 32) | Tlo;
  if (tid == 0) publish(ws, OFF_F0 + b * 128, T);
  STAMP(1);
#pragma unroll
  for (int a = 0; a < 3; ++a) {
    if (a < 2 || w < 4) {
      float rstd, nb;
      row_stats(xr[a], rstd, nb);
      const f32x2 r2 = {rstd, rstd}, n2 = {nb, nb};
#pragma unroll
      for (int m = 0; m < 5; ++m) {
#pragma unroll
        for (int e = 0; e < 4; ++e) xr[a][m][e] = pk(fma2(unpk(xr[a][m][e]), r2, n2));
        asm volatile("" : "+v"(xr[a][m]));
      }
    }
  }
  STAMP(2);
  if (w < 4) poll(ws, OFF_F0 + w * 64 * 128, 64, lane, T);
  bar();
  STAMP(3);
  if (b == 0 && tid == 0) publish(ws, OFF_E, T);

  v4u pv1[4];
  if (tid < 320) {
#pragma unroll
    for (int k = 0; k < 4; ++k) pv1[k] = ldw(rs, tid * 16, OFF_MODP + k * 15360);
  }
  constexpr int R1 = 8;
  v4u wr[R1][2];
  const u32 wq0 = OFF_WQ + (g * 15 + 2 * w) * 20480, wq1 = OFF_WQ + (g * 15 + (w < 7 ? 2 * w + 1 : 14)) * 20480;
#pragma unroll
  for (int j = 0; j < R1; ++j) { wr[j][0] = ldw(rs, l16 + (j & 3) * 1024, wq0 + (j >> 2) * 4096); wr[j][1] = ldw(rs, l16 + (j & 3) * 1024, wq1 + (j >> 2) * 4096); }
  SCHED();
  if (tid < 320) {
    f32x4 s = {0.f, 0.f, 0.f, 0.f};
#pragma unroll
    for (int k = 0; k < 4; ++k) s += __builtin_bit_cast(f32x4, pv1[k]);
    if (tid >= 160) s += 1.f;
    *(LAS f32x4*)(L + L_VEC + tid * 16) = s;
  }
  bar();
  f32x4 acc1[5][2];
#pragma unroll
  for (int i = 0; i < 5; ++i) { acc1[i][0] = f32x4{0.f, 0.f, 0.f, 0.f}; acc1[i][1] = f32x4{0.f, 0.f, 0.f, 0.f}; }
  const int rd_even = g4 * 256 + ((t16 ^ g4) * 16), rd_odd = rd_even ^ 64;
  auto prep1 = [&](int m, int a) {
    const int k0 = (t16 + 16 * m) * 8;
    const f32x4 s0 = ldsf4(L, L_VEC + k0 * 4), s1 = ldsf4(L, L_VEC + k0 * 4 + 16);
    const f32x4 c0 = ldsf4(L, L_VEC + 2560 + k0 * 4), c1 = ldsf4(L, L_VEC + 2560 + k0 * 4 + 16);
    const int grp = w + 8 * a, tt = grp >> 2, t = 4 * (grp & 3) + g4;
    const v4u r = xr[a][m];
    v4u o;
    o.x = pk(fma2(unpk(r.x), c0.xy, s0.xy));
    o.y = pk(fma2(unpk(r.y), c0.zw, s0.zw));
    o.z = pk(fma2(unpk(r.z), c1.xy, s1.xy));
    o.w = pk(fma2(unpk(r.w), c1.zw, s1.zw));
    *(LAS v4u*)(L + L_A + tt * 20480 + (4 * m + (t16 >> 2)) * 1024 + (t16 & 3) * 256 + ((t ^ (t16 & 7)) * 16)) = o;
  };
  prep1(0, 0); prep1(0, 1);
  if (w < 4) prep1(0, 2);
  bar();
  STAMP(4);
  {
    v4u af[2][5];
#pragma unroll
    for (int tt = 0; tt < 5; ++tt) af[0][tt] = lds128(L, L_A + tt * 20480 + rd_even);
#pragma unroll
    for (int j = 0; j < 20; ++j) {
      const int m = j >> 2, q = j & 3;
      if (j + 1 < 20) {
#pragma unroll
        for (int tt = 0; tt < 5; ++tt) af[(j + 1) & 1][tt] = lds128(L, L_A + tt * 20480 + (j + 1) * 1024 + (((j + 1) & 1) ? rd_odd : rd_even));
      }
      const v4u wa = wr[j % R1][0], wb = wr[j % R1][1];
      if (j + R1 < 20) {
        wr[j % R1][0] = ldw(rs, l16 + ((j + R1) & 3) * 1024, wq0 + ((j + R1) >> 2) * 4096);
        wr[j % R1][1] = ldw(rs, l16 + ((j + R1) & 3) * 1024, wq1 + ((j + R1) >> 2) * 4096);
      }
      if (m < 4 && q < 2) prep1(m + 1, q);
#pragma unroll
      for (int tt = 0; tt < 5; ++tt) {
        acc1[tt][0] = mfma(wa, af[j & 1][tt], acc1[tt][0]);
        acc1[tt][1] = mfma(wb, af[j & 1][tt], acc1[tt][1]);
      }
      SGB(0x100, 9); SGB(0x020, 2);
#pragma unroll
      for (int i = 0; i < 10; ++i) { SGB(0x008, 1); SGB(0x002, 3); }
      SCHED();
      if (m < 4 && q == 2) {
        if (w < 4) prep1(m + 1, 2);
        bar();
      }
    }
  }
  bar();
  STAMP(5);
#pragma unroll
  for (int tt = 0; tt < 5; ++tt)
#pragma unroll
    for (int c = 0; c < 2; ++c)
      if (c == 0 || w < 7)
        *(LAS v2u*)(L + L_A + (tt * 16 + t16) * QS + (2 * w + c) * 32 + g4 * 8) =
            v2u{pack2(acc1[tt][c].x, acc1[tt][c].y), pack2(acc1[tt][c].z, acc1[tt][c].w)};
  bar();
  STAMP(6);
  {
    const int h = w & 3, par = w >> 2;
    const float cs = 0.22360679774997896f * 1.4426950408889634f;
#pragma unroll
    for (int qt = 0; qt < 2; ++qt) {
      const int qrow = 2 * (8 + 16 * qt + t16) + par;
      const int qo = L_A + qrow * QS + 40 * h + (g4 < 3 ? 16 * g4 : 0);
      v2u qlo = lds64(L, qo), qhi = lds64(L, qo + 8);
      if (g4 == 3) qlo = v2u{0, 0};
      if (g4 >= 2) qhi = v2u{0, 0};
      const v4u qf = {qlo.x, qlo.y, qhi.x, qhi.y};
      f32x4 sc[2];
#pragma unroll
      for (int kt = 0; kt < 2; ++kt) {
        int ki = 16 * qt + 16 * kt + t16;
        ki = ki > 39 ? 39 : ki;
        const int ko = L_A + (2 * ki + par) * QS + 160 + 40 * h + 16 * g4;
        const v2u klo = lds64(L, ko), khi = lds64(L, ko + 8);
        sc[kt] = mfma(v4u{klo.x, klo.y, khi.x, khi.y}, qf, f32x4{0.f, 0.f, 0.f, 0.f});
      }
      const int vq = t16 >> 2, vp = lane & 3;
      int ka2 = 16 * qt + 4 * g4 + vq, kb = ka2 + 16;
      kb = kb > 39 ? 39 : kb;
      const int va = L_A + (2 * ka2 + par) * QS + 320 + 40 * h, vb = L_A + (2 * kb + par) * QS + 320 + 40 * h;
      const v2u a0 = ldstr(L, va + 8 * vp), b0 = ldstr(L, vb + 8 * vp);
      const v2u a1 = ldstr(L, va + 32), b1v = ldstr(L, vb + 32);
      float mx = -3.0e38f;
#pragma unroll
      for (int kt = 0; kt < 2; ++kt)
#pragma unroll
        for (int r = 0; r < 4; ++r) {
          const int kr = 16 * kt + 4 * g4 + r;
          bool ok = (unsigned)(kr - t16 - 1) < 8u;
          if (qt == 0) ok = ok && (p > 0 || kr >= 8);
          sc[kt][r] = ok ? sc[kt][r] : -3.0e38f;
          mx = fmaxf(mx, sc[kt][r]);
        }
      mx = x32_max(x16_max(mx));
      float sum = 0.f;
#pragma unroll
      for (int kt = 0; kt < 2; ++kt)
#pragma unroll
        for (int r = 0; r < 4; ++r) { sc[kt][r] = __builtin_amdgcn_exp2f((sc[kt][r] - mx) * cs); sum += sc[kt][r]; }
      sum = x32_sum(x16_sum(sum));
      const float inv = __builtin_amdgcn_rcpf(sum);
      const v4u pf4 = {pack2(sc[0].x * inv, sc[0].y * inv), pack2(sc[0].z * inv, sc[0].w * inv), pack2(sc[1].x * inv, sc[1].y * inv), pack2(sc[1].z * inv, sc[1].w * inv)};
      const f32x4 o0 = mfma(v4u{a0.x, a0.y, b0.x, b0.y}, pf4, f32x4{0.f, 0.f, 0.f, 0.f});
      const f32x4 o1 = mfma(v4u{a1.x, a1.y, b1v.x, b1v.y}, pf4, f32x4{0.f, 0.f, 0.f, 0.f});
      const int orow = L_OB + (par * 32 + 16 * qt + t16) * OS + 40 * h;
      *(LAS v2u*)(L + orow + 8 * g4) = v2u{pack2(o0.x, o0.y), pack2(o0.z, o0.w)};
      if (g4 == 0) *(LAS v2u*)(L + orow + 32) = v2u{pack2(o1.x, o1.y), pack2(o1.z, o1.w)};
    }
  }
  bar();
  STAMP(21);
  {
    const int tok = 2 * (lane & 31) + (lane >> 5);
#pragma unroll
    for (int i = 0; i < 2; ++i) {
      const int u = w + 8 * i;
      if (u < 10) {
        const int ch = 10 * g + u;
        const v4u v = lds128(L, L_OB + lane * OS + 16 * u);
        stw(rs, v, (tok >> 4) * 20480 + (tok & 15) * 16, OFF_O + p * 81920 + (ch >> 2) * 1024 + (ch & 3) * 256);
      }
    }
  }
  drain();
  bar();
  if (tid == 0) publish(ws, OFF_F1 + (p * 8 + g) * 128, T);
  STAMP(7);

  const int s4 = w & 3, kh = w >> 2;
  const int tokg = 64 * p + 16 * s4 + t16;
  const int fo = kh == 0 ? 8 * g4 : 32 + 8 * g4;
  const v4u xa = *(const GAS v4u*)(x + (u32)(tokg * 1280 + (80 * g + fo) * 2));
  const v2u xb = *(const GAS v2u*)(x + (u32)(tokg * 1280 + (80 * g + 64 + 4 * g4) * 2));
  if (tid < 320) {
    f32x4 s = {0.f, 0.f, 0.f, 0.f};
    v4u pv[4];
#pragma unroll
    for (int k = 0; k < 4; ++k) pv[k] = ldw(rs, tid * 16, OFF_MODP + k * 15360 + 1920 * 4);
#pragma unroll
    for (int k = 0; k < 4; ++k) s += __builtin_bit_cast(f32x4, pv[k]);
    if (tid >= 160) s += 1.f;
    *(LAS f32x4*)(L + L_VEC + tid * 16) = s;
  } else if (tid < 360) {
    const int i = tid - 320, which = i / 20, c4 = i % 20;
    f32x4 s = {0.f, 0.f, 0.f, 0.f};
    v4u pv[4];
#pragma unroll
    for (int k = 0; k < 4; ++k) pv[k] = ldw(rs, (which ? 3200 : 1280) * 4 + c4 * 16, OFF_MODP + k * 15360 + g * 320);
#pragma unroll
    for (int k = 0; k < 4; ++k) s += __builtin_bit_cast(f32x4, pv[k]);
    *(LAS f32x4*)(L + L_SM + which * 320 + c4 * 16) = s;
  } else if (tid >= 384 && tid < 404) {
    const int c4 = tid - 384;
    const v2u v = *(const GAS v2u*)(b2 + (80 * g + 4 * c4) * 2);
    *(LAS f32x4*)(L + L_SM + 640 + c4 * 16) = f32x4{blo(v.x), bhi(v.x), blo(v.y), bhi(v.y)};
  }
  if (w < 7) {
#pragma unroll
    for (int i = 0; i < 4; ++i) {
      const int gi = w + 7 * i;
      if (gi < 25) { dma<4>(rs, l16, OFF_WO + g * 102400 + gi * 4096, L_A + gi * 4096); VMCNT(4); }
    }
    VMCNT(0);
  } else {
    poll(ws, OFF_F1 + p * 8 * 128, 8, lane, T);
  }
  bar();
  STAMP(8);
  v4u oa[10];
#pragma unroll
  for (int i = 0; i < 10; ++i) oa[i] = ldw(rs, l16 + (i & 3) * 1024, OFF_O + (p * 4 + s4) * 20480 + kh * 10240 + (i >> 2) * 4096);
  SCHED();
  f32x4 acc2[5];
#pragma unroll
  for (int i = 0; i < 5; ++i) acc2[i] = f32x4{0.f, 0.f, 0.f, 0.f};
  {
    v4u wf[2][5];
#pragma unroll
    for (int ct = 0; ct < 5; ++ct) wf[0][ct] = lds128(L, L_A + ((10 * kh) * 5 + ct) * 1024 + l16);
#pragma unroll
    for (int i = 0; i < 10; ++i) {
      if (i + 1 < 10) {
#pragma unroll
        for (int ct = 0; ct < 5; ++ct) wf[(i + 1) & 1][ct] = lds128(L, L_A + ((10 * kh + i + 1) * 5 + ct) * 1024 + l16);
      }
      SCHED();
#pragma unroll
      for (int ct = 0; ct < 5; ++ct) acc2[ct] = mfma(wf[i & 1][ct], oa[i], acc2[ct]);
      SCHED();
    }
  }
  if (kh == 0) {
    *(LAS f32x4*)(L + L_RED + (s4 * 5 + 2) * 1024 + l16) = acc2[2];
    *(LAS f32x4*)(L + L_RED + (s4 * 5 + 3) * 1024 + l16) = acc2[3];
  } else {
    *(LAS f32x4*)(L + L_RED + (s4 * 5 + 0) * 1024 + l16) = acc2[0];
    *(LAS f32x4*)(L + L_RED + (s4 * 5 + 1) * 1024 + l16) = acc2[1];
    *(LAS f32x4*)(L + L_RED + (s4 * 5 + 4) * 1024 + l16) = acc2[4];
  }
  bar();
  STAMP(9);
  v4u x1a; v2u x1b = {0, 0};
  {
    const int c0 = kh == 0 ? 0 : 2;
    const f32x4 u0 = (kh == 0 ? acc2[0] : acc2[2]) + ldsf4(L, L_RED + (s4 * 5 + c0) * 1024 + l16);
    const f32x4 u1 = (kh == 0 ? acc2[1] : acc2[3]) + ldsf4(L, L_RED + (s4 * 5 + c0 + 1) * 1024 + l16);
    const f32x4 g0 = ldsf4(L, L_SM + fo * 4), g1 = ldsf4(L, L_SM + fo * 4 + 16);
    x1a.x = pk(fma2(g0.xy, u0.xy, unpk(xa.x)));
    x1a.y = pk(fma2(g0.zw, u0.zw, unpk(xa.y)));
    x1a.z = pk(fma2(g1.xy, u1.xy, unpk(xa.z)));
    x1a.w = pk(fma2(g1.zw, u1.zw, unpk(xa.w)));
    stw(rs, x1a, tokg * 1280 + (80 * g + fo) * 2, OFF_X1);
    if (kh == 0) {
      const f32x4 u2 = acc2[4] + ldsf4(L, L_RED + (s4 * 5 + 4) * 1024 + l16);
      const f32x4 g2 = ldsf4(L, L_SM + (64 + 4 * g4) * 4);
      x1b.x = pk(fma2(g2.xy, u2.xy, unpk(xb.x)));
      x1b.y = pk(fma2(g2.zw, u2.zw, unpk(xb.y)));
      __builtin_amdgcn_raw_buffer_store_b64(x1b, rs, tokg * 1280 + (80 * g + 64 + 4 * g4) * 2, OFF_X1, SC1);
    }
  }
  drain();
  bar();
  if (tid == 0) publish(ws, OFF_F2 + (p * 8 + g) * 128, T);
  STAMP(10);

  const int j0 = g < 7 ? 7 * g : 48, nk = g < 6 ? 7 : 6;
  const bool act3 = w < nk;
  const int jb = j0 + (act3 ? w : 0);
  constexpr int R3 = 8;
  v4u w3[R3][2];
  const u32 w1o = OFF_W1 + jb * 40960;
  v4u b1v = {0, 0, 0, 0};
  if (act3) {
#pragma unroll
    for (int j = 0; j < 4; ++j) { w3[j][0] = ldw(rs, l16 + (j & 1) * 2048, w1o + (j >> 1) * 4096); w3[j][1] = ldw(rs, l16 + (j & 1) * 2048 + 1024, w1o + (j >> 1) * 4096); }
    b1v = *(const GAS v4u*)(b1 + (32 * jb + 8 * g4) * 2);
  }
  SCHED();
  if (w == 7) poll(ws, OFF_F2 + p * 8 * 128, 8, lane, T);
  bar();
  STAMP(11);
  v4u yr[2][5];
#pragma unroll
  for (int a = 0; a < 2; ++a)
#pragma unroll
    for (int m = 0; m < 5; ++m) yr[a][m] = ldw(rs, (64 * p + 8 * w + 4 * a + g4) * 1280 + (t16 + 16 * m) * 16, OFF_X1);
  if (act3) {
#pragma unroll
    for (int j = 4; j < R3; ++j) { w3[j][0] = ldw(rs, l16 + (j & 1) * 2048, w1o + (j >> 1) * 4096); w3[j][1] = ldw(rs, l16 + (j & 1) * 2048 + 1024, w1o + (j >> 1) * 4096); }
  }
  SCHED();
#pragma unroll
  for (int a = 0; a < 2; ++a) {
    asm volatile("" ::: "memory");
    SCHED();
    float rstd, nb;
    row_stats(yr[a], rstd, nb);
    const f32x2 r2 = {rstd, rstd}, n2 = {nb, nb};
#pragma unroll
    for (int m = 0; m < 5; ++m) {
      const int k0 = (t16 + 16 * m) * 8;
      const f32x4 s0 = ldsf4(L, L_VEC + k0 * 4), s1 = ldsf4(L, L_VEC + k0 * 4 + 16);
      const f32x4 c0 = ldsf4(L, L_VEC + 2560 + k0 * 4), c1 = ldsf4(L, L_VEC + 2560 + k0 * 4 + 16);
      yr[a][m].x = pk(fma2(fma2(unpk(yr[a][m].x), r2, n2), c0.xy, s0.xy));
      yr[a][m].y = pk(fma2(fma2(unpk(yr[a][m].y), r2, n2), c0.zw, s0.zw));
      yr[a][m].z = pk(fma2(fma2(unpk(yr[a][m].z), r2, n2), c1.xy, s1.xy));
      yr[a][m].w = pk(fma2(fma2(unpk(yr[a][m].w), r2, n2), c1.zw, s1.zw));
    }
    row_stats(yr[a], rstd, nb);
    const f32x2 r3 = {rstd, rstd}, n3 = {nb, nb};
    const int row = 8 * w + 4 * a + g4, tt = row >> 4, t = row & 15;
#pragma unroll
    for (int m = 0; m < 5; ++m) {
      const v4u o = {pk(fma2(unpk(yr[a][m].x), r3, n3)), pk(fma2(unpk(yr[a][m].y), r3, n3)),
                     pk(fma2(unpk(yr[a][m].z), r3, n3)), pk(fma2(unpk(yr[a][m].w), r3, n3))};
      *(LAS v4u*)(L + L_A + tt * 20480 + (4 * m + (t16 >> 2)) * 1024 + (t16 & 3) * 256 + ((t ^ (t16 & 7)) * 16)) = o;
    }
  }
  bar();
  STAMP(12);
  const u32 w2g = OFF_W2 + g * 54 * 5120;
  f32x4 acc3[4][2];
#pragma unroll
  for (int i = 0; i < 4; ++i) { acc3[i][0] = f32x4{0.f, 0.f, 0.f, 0.f}; acc3[i][1] = f32x4{0.f, 0.f, 0.f, 0.f}; }
  if (act3) {
    v4u af[2][4];
#pragma unroll
    for (int tt = 0; tt < 4; ++tt) af[0][tt] = lds128(L, L_A + tt * 20480 + rd_even);
#pragma unroll
    for (int j = 0; j < 20; ++j) {
      if (j + 1 < 20) {
#pragma unroll
        for (int tt = 0; tt < 4; ++tt) af[(j + 1) & 1][tt] = lds128(L, L_A + tt * 20480 + (j + 1) * 1024 + (((j + 1) & 1) ? rd_odd : rd_even));
      }
      const v4u wa = w3[j % R3][0], wb = w3[j % R3][1];
      if (j + R3 < 20) {
        w3[j % R3][0] = ldw(rs, l16 + ((j + R3) & 1) * 2048, w1o + ((j + R3) >> 1) * 4096);
        w3[j % R3][1] = ldw(rs, l16 + ((j + R3) & 1) * 2048 + 1024, w1o + ((j + R3) >> 1) * 4096);
      }
      SCHED();
#pragma unroll
      for (int tt = 0; tt < 4; ++tt) {
        acc3[tt][0] = mfma(wa, af[j & 1][tt], acc3[tt][0]);
        acc3[tt][1] = mfma(wb, af[j & 1][tt], acc3[tt][1]);
      }
      SCHED();
    }
  } else if (w == 7) {
#pragma unroll 1
    for (int i = 0; i < 17; ++i) { dma<4>(rs, l16, w2g + (27 * 5 + 5 + 4 * i) * 1024, 81920 + i * 4096); VMCNT(8); }
    dma<2>(rs, l16, w2g + (27 * 5 + 5 + 68) * 1024, 81920 + 68 * 1024);
  }
  bar();
  STAMP(13);
  if (act3) {
    const float bb[8] = {blo(b1v.x), bhi(b1v.x), blo(b1v.y), bhi(b1v.y), blo(b1v.z), bhi(b1v.z), blo(b1v.w), bhi(b1v.w)};
    const float k1c = -2.f * 0.7978845608028654f * 1.4426950408889634f, k3c = k1c * 0.044715f;
#pragma unroll
    for (int tt = 0; tt < 4; ++tt) {
      float hv[8];
#pragma unroll
      for (int e = 0; e < 8; ++e) {
        const float v = acc3[tt][e >> 2][e & 3] + bb[e];
        const float ex = __builtin_amdgcn_exp2f(v * (k1c + k3c * v * v));
        hv[e] = v * __builtin_amdgcn_rcpf(1.f + ex);
      }
      stw(rs, v4u{pack2(hv[0], hv[1]), pack2(hv[2], hv[3]), pack2(hv[4], hv[5]), pack2(hv[6], hv[7])}, l16, OFF_HH + ((p * 4 + tt) * 54 + jb) * 1024);
    }
  } else if (w == 7) {
#pragma unroll 1
    for (int i = 0; i < 18; ++i) dma<4>(rs, l16, w2g + 4 * i * 1024, i * 4096);
    dma<3>(rs, l16, w2g + 72 * 1024, 72 * 1024);
    dma<4>(rs, l16, w2g + 27 * 5 * 1024, 76800);
    dma<1>(rs, l16, w2g + (27 * 5 + 4) * 1024, 76800 + 4096);
  }
  if (act3) drain();
  bar();
  if (tid == 0) publish(ws, OFF_F3 + (p * 8 + g) * 128, T);
  STAMP(14);

  const u32 w2o = w2g + 27 * kh * 5120;
  const u32 ring = kh * 76800;
  if (w == 7) poll(ws, OFF_F3 + p * 8 * 128, 8, lane, T);
  bar();
  STAMP(15);
  v4u ha[27];
#pragma unroll
  for (int i = 0; i < 27; ++i) ha[i] = ldw(rs, l16 + (i & 3) * 1024, OFF_HH + ((p * 4 + s4) * 54 + 27 * kh) * 1024 + (i >> 2) * 4096);
  SCHED();
  f32x4 acc4[5];
#pragma unroll
  for (int i = 0; i < 5; ++i) acc4[i] = f32x4{0.f, 0.f, 0.f, 0.f};
  {
    v4u wf[2][5];
#pragma unroll
    for (int ct = 0; ct < 5; ++ct) wf[0][ct] = lds128(L, ring + ct * 1024 + l16);
#pragma unroll
    for (int i = 0; i < 27; ++i) {
      const int n = i / 3;
      if (i + 1 < 27) {
#pragma unroll
        for (int ct = 0; ct < 5; ++ct) wf[(i + 1) & 1][ct] = lds128(L, ring + (((i + 1) % 15) * 5 + ct) * 1024 + l16);
      }
      SCHED();
#pragma unroll
      for (int ct = 0; ct < 5; ++ct) acc4[ct] = mfma(wf[i & 1][ct], ha[i], acc4[ct]);
      SCHED();
      if (i % 3 == 2) {
        if (n == 3 || n == 4) VMCNT(6);
        if (n == 5) VMCNT(3);
        if (n == 6) VMCNT(0);
        bar();
        if (n < 4) {
          if (s4 < 3) dma<4>(rs, l16, w2o + (75 + 15 * n + 4 * s4) * 1024, ring + (15 * n + 4 * s4) * 1024);
          else dma<3>(rs, l16, w2o + (75 + 15 * n + 12) * 1024, ring + (15 * n + 12) * 1024);
        }
      }
    }
  }
  STAMP(16);
  if (kh == 0) {
    *(LAS f32x4*)(L + L_RED + (s4 * 5 + 2) * 1024 + l16) = acc4[2];
    *(LAS f32x4*)(L + L_RED + (s4 * 5 + 3) * 1024 + l16) = acc4[3];
  } else {
    *(LAS f32x4*)(L + L_RED + (s4 * 5 + 0) * 1024 + l16) = acc4[0];
    *(LAS f32x4*)(L + L_RED + (s4 * 5 + 1) * 1024 + l16) = acc4[1];
    *(LAS f32x4*)(L + L_RED + (s4 * 5 + 4) * 1024 + l16) = acc4[4];
  }
  bar();
  {
    const int c0 = kh == 0 ? 0 : 2;
    const f32x4 u0 = (kh == 0 ? acc4[0] : acc4[2]) + ldsf4(L, L_RED + (s4 * 5 + c0) * 1024 + l16);
    const f32x4 u1 = (kh == 0 ? acc4[1] : acc4[3]) + ldsf4(L, L_RED + (s4 * 5 + c0 + 1) * 1024 + l16);
    const f32x4 g0 = ldsf4(L, L_SM + 320 + fo * 4), g1 = ldsf4(L, L_SM + 320 + fo * 4 + 16);
    const f32x4 e0 = ldsf4(L, L_SM + 640 + fo * 4), e1 = ldsf4(L, L_SM + 640 + fo * 4 + 16);
    v4u o;
    o.x = pk(fma2(g0.xy, u0.xy + e0.xy, unpk(x1a.x)));
    o.y = pk(fma2(g0.zw, u0.zw + e0.zw, unpk(x1a.y)));
    o.z = pk(fma2(g1.xy, u1.xy + e1.xy, unpk(x1a.z)));
    o.w = pk(fma2(g1.zw, u1.zw + e1.zw, unpk(x1a.w)));
    *(GAS v4u*)(out + (u32)(tokg * 1280 + (80 * g + fo) * 2)) = o;
    if (kh == 0) {
      const f32x4 u2 = acc4[4] + ldsf4(L, L_RED + (s4 * 5 + 4) * 1024 + l16);
      const f32x4 g2 = ldsf4(L, L_SM + 320 + (64 + 4 * g4) * 4), e2 = ldsf4(L, L_SM + 640 + (64 + 4 * g4) * 4);
      const v2u o2 = {pk(fma2(g2.xy, u2.xy + e2.xy, unpk(x1b.x))), pk(fma2(g2.zw, u2.zw + e2.zw, unpk(x1b.y)))};
      *(GAS v2u*)(out + (u32)(tokg * 1280 + (80 * g + 64 + 4 * g4) * 2)) = o2;
    }
  }
  STAMP(17);
  if (b == 0 && w == 0) {
    const u64 until = __builtin_amdgcn_s_memrealtime() + 0 * (probe_a - probe_k);
    while (__builtin_amdgcn_s_memrealtime() < until) __builtin_amdgcn_s_sleep(2);
  }
  asm volatile("fused_pf_end:" ::: "memory");
}

extern "C" void kernel_launch(void* const* d_in, const int* in_sizes, int n_in, void* d_out, int out_size, void* d_ws, size_t ws_size,
                              hipStream_t stream) {
  (void)in_sizes; (void)n_in; (void)out_size; (void)ws_size;
  (void)hipFuncSetAttribute((const void*)fused_block, hipFuncAttributeMaxDynamicSharedMemorySize, LDS_BYTES);
  fused_block<<<NWG, NTHR, LDS_BYTES, stream>>>((const u16*)d_in[0], (const u16*)d_in[1], (const u16*)d_in[2], (const u16*)d_in[4],
                                                (const u16*)d_in[5], (const u16*)d_in[6], (const u16*)d_in[7], (const u16*)d_in[8],
                                                (const u16*)d_in[9], (u16*)d_out, (unsigned char*)d_ws);
}
```

```cpp
#include <hip/hip_runtime.h>

typedef unsigned short u16;
typedef unsigned int u32;
typedef unsigned long long u64;
typedef u32 v4u __attribute__((ext_vector_type(4)));
typedef u32 v2u __attribute__((ext_vector_type(2)));
typedef float f32x4 __attribute__((ext_vector_type(4)));
typedef float f32x2 __attribute__((ext_vector_type(2)));
typedef short s16x8 __attribute__((ext_vector_type(8)));
typedef short s16x4 __attribute__((ext_vector_type(4)));
typedef __bf16 bf16x2 __attribute__((ext_vector_type(2)));
#define GAS __attribute__((address_space(1)))
#define LAS __attribute__((address_space(3)))
#define DI __device__ __forceinline__
typedef __amdgpu_buffer_rsrc_t rsrc_t;

#define ABL_NOFILL1 0
#define ABL_NOPREP 0
#define ABL_NOBAR1 0
#define ABL_NOFILL3 0
#define ABL_NOSTAGE3 0
#define ABL_NOREFILL4 0
#define ABL_NOACT4 0
#define ABL_P 0
#define ABL_NORD1 0
#define XN 2

constexpr int NWG = 256, NTHR = 512;
constexpr u32 OFF_E = 0, OFF_F0 = 0x1000, OFF_F1 = 0xA000, OFF_F2 = 0x12000, OFF_F3 = 0x1A000;
constexpr u32 OFF_MODP = 0x30000, OFF_WQ = 0x40000, OFF_WO = 0x300000, OFF_W1 = 0x400000, OFF_W2 = 0x700000;
constexpr u32 OFF_O = 0xA00000, OFF_XC = 0xD00000, OFF_HH = 0x1000000, OFF_MOM = 0x16C0000, OFF_UVZ = 0x17C0000, WS_USED = 0x1800000;
constexpr u32 OFF_FU = 0x22000, OFF_F0B = 0x26000;
constexpr u32 OFF_DIAG = 0x1800000;
constexpr int L_A = 0, L_OB = 39424, L_WO = 51200, L_JOB = 102400, L_GV = 143360, L_RED = 0, L_RED4 = 102400, L_SM = 153600, L_VEC = 154624, LDS_BYTES = 163840;
constexpr int L_SPK = 154624, L_CPK = 155904, L_ONE = 157184, L_CONST = 157216, L_CF = 157312, L_ROWC = 157696, L_UVZ = 158720;
constexpr int QS = 488;
constexpr int OS = 176;
constexpr int SPIN_CAP = 1 << 14;
constexpr int SC1 = 16;

DI f32x4 mfma(v4u a, v4u b, f32x4 c) {
  return __builtin_amdgcn_mfma_f32_16x16x32_bf16(__builtin_bit_cast(s16x8, a), __builtin_bit_cast(s16x8, b), c, 0, 0, 0);
}
DI u32 pack2(float a, float b) { f32x2 v = {a, b}; return __builtin_bit_cast(u32, __builtin_convertvector(v, bf16x2)); }
DI float blo(u32 u) { return __builtin_bit_cast(float, u << 16); }
DI float bhi(u32 u) { return __builtin_bit_cast(float, u & 0xffff0000u); }
DI f32x2 unpk(u32 u) { return f32x2{blo(u), bhi(u)}; }
DI f32x2 fma2(f32x2 a, f32x2 b, f32x2 c) { return __builtin_elementwise_fma(a, b, c); }
DI u32 pk(f32x2 v) { return __builtin_bit_cast(u32, __builtin_convertvector(v, bf16x2)); }
DI float dot2(u32 a, u32 b, float c) { return __builtin_amdgcn_fdot2_f32_bf16(__builtin_bit_cast(bf16x2, a), __builtin_bit_cast(bf16x2, b), c, false); }
DI float fma1(float a, float b, float c) { float r; asm("v_fma_f32 %0, %1, %2, %3" : "=v"(r) : "v"(a), "v"(b), "v"(c)); return r; }
DI float bf2f(u16 v) { return __builtin_bit_cast(float, (u32)v << 16); }
template <int CTRL> DI float dpp_add(float v) {
  return v + __builtin_bit_cast(float, __builtin_amdgcn_update_dpp(0, __builtin_bit_cast(int, v), CTRL, 0xf, 0xf, true));
}
DI float row16_sum(float v) { v = dpp_add<0x128>(v); v = dpp_add<0x124>(v); v = dpp_add<0x122>(v); return dpp_add<0x121>(v); }
DI float x16_sum(float v) {
  auto r = __builtin_amdgcn_permlane16_swap(__builtin_bit_cast(u32, v), __builtin_bit_cast(u32, v), false, false);
  const u32 a = r[0], c = r[1];
  return __builtin_bit_cast(float, a) + __builtin_bit_cast(float, c);
}
DI float x32_sum(float v) {
  auto r = __builtin_amdgcn_permlane32_swap(__builtin_bit_cast(u32, v), __builtin_bit_cast(u32, v), false, false);
  const u32 a = r[0], c = r[1];
  return __builtin_bit_cast(float, a) + __builtin_bit_cast(float, c);
}
DI float x16_max(float v) {
  auto r = __builtin_amdgcn_permlane16_swap(__builtin_bit_cast(u32, v), __builtin_bit_cast(u32, v), false, false);
  const u32 a = r[0], c = r[1];
  return fmaxf(__builtin_bit_cast(float, a), __builtin_bit_cast(float, c));
}
DI float x32_max(float v) {
  auto r = __builtin_amdgcn_permlane32_swap(__builtin_bit_cast(u32, v), __builtin_bit_cast(u32, v), false, false);
  const u32 a = r[0], c = r[1];
  return fmaxf(__builtin_bit_cast(float, a), __builtin_bit_cast(float, c));
}
DI float swap16_add(float a, float b) {
  auto r = __builtin_amdgcn_permlane16_swap(__builtin_bit_cast(u32, a), __builtin_bit_cast(u32, b), false, false);
  const u32 x = r[0], y = r[1];
  return __builtin_bit_cast(float, x) + __builtin_bit_cast(float, y);
}
DI float swap32_add(float a, float b) {
  auto r = __builtin_amdgcn_permlane32_swap(__builtin_bit_cast(u32, a), __builtin_bit_cast(u32, b), false, false);
  const u32 x = r[0], y = r[1];
  return __builtin_bit_cast(float, x) + __builtin_bit_cast(float, y);
}
template <int V> struct IntC { static constexpr int value = V; };
template <bool V> struct BoolC { static constexpr bool value = V; };
template <int N, int I = 0, class F> DI void static_for(F&& f) {
  if constexpr (I < N) { f(IntC<I>{}); static_for<N, I + 1>(f); }
}
DI void bar() { asm volatile("s_waitcnt lgkmcnt(0)\n\ts_barrier" ::: "memory"); }
DI void drain() { asm volatile("s_waitcnt vmcnt(0)" ::: "memory"); }
#define SCHED() __builtin_amdgcn_sched_barrier(0)
#define SGB(mask, n) __builtin_amdgcn_sched_group_barrier(mask, n, 0)
#define VMCNT(n) asm volatile("s_waitcnt vmcnt(" #n ")" ::: "memory")
DI v4u ldw(rsrc_t rs, u32 voff, u32 soff) { return __builtin_amdgcn_raw_buffer_load_b128(rs, voff, soff, SC1); }
DI void stw(rsrc_t rs, v4u v, u32 voff, u32 soff) { __builtin_amdgcn_raw_buffer_store_b128(v, rs, voff + soff, 0, SC1); }
DI v4u lds128(LAS unsigned char* L, int off) { return *(LAS v4u*)(L + off); }
DI f32x4 ldsf4(LAS unsigned char* L, int off) { return *(LAS f32x4*)(L + off); }
DI v2u lds64(LAS unsigned char* L, int off) { return *(LAS v2u*)(L + off); }
DI v2u ldstr(LAS unsigned char* L, int off) {
  return __builtin_bit_cast(v2u, __builtin_amdgcn_ds_read_tr16_b64_v4i16((LAS s16x4*)(L + off)));
}
template <int N> DI void dma(rsrc_t rs, u32 voff, u32 soff, u32 ldsoff) {
#define DMA_I(o) "buffer_load_dwordx4 %1, %2, %3 offen offset:" #o " sc1 lds\n\t"
#define DMA_ARGS :: "s"(ldsoff), "v"(voff), "s"(rs), "s"(soff) : "memory"
  if constexpr (N == 4) asm volatile("s_mov_b32 m0, %0\n\ts_nop 0\n\t" DMA_I(0) DMA_I(1024) DMA_I(2048) DMA_I(3072) DMA_ARGS);
  else if constexpr (N == 3) asm volatile("s_mov_b32 m0, %0\n\ts_nop 0\n\t" DMA_I(0) DMA_I(1024) DMA_I(2048) DMA_ARGS);
  else if constexpr (N == 2) asm volatile("s_mov_b32 m0, %0\n\ts_nop 0\n\t" DMA_I(0) DMA_I(1024) DMA_ARGS);
  else asm volatile("s_mov_b32 m0, %0\n\ts_nop 0\n\t" DMA_I(0) DMA_ARGS);
}
DI void poll(unsigned char* ws, u32 off, int n, int lane, u64 T) {
  const GAS u64* f = (const GAS u64*)(ws + off + (lane < n ? lane : 0) * 128);
#pragma unroll 1
  for (int spins = 0; spins < SPIN_CAP; ++spins) {
    const u64 v = __hip_atomic_load(f, __ATOMIC_RELAXED, __HIP_MEMORY_SCOPE_AGENT);
    if (__all(v == T)) break;
    __builtin_amdgcn_s_sleep(1);
  }
  __builtin_amdgcn_fence(__ATOMIC_ACQUIRE, "wavefront");
}
DI void poll256(unsigned char* ws, u32 off, int lane, u64 T) {
  const GAS u64* f = (const GAS u64*)(ws + off + lane * 128);
#pragma unroll 1
  for (int spins = 0; spins < SPIN_CAP; ++spins) {
    const u64 v0 = __hip_atomic_load(f, __ATOMIC_RELAXED, __HIP_MEMORY_SCOPE_AGENT);
    const u64 v1 = __hip_atomic_load(f + 1024, __ATOMIC_RELAXED, __HIP_MEMORY_SCOPE_AGENT);
    const u64 v2 = __hip_atomic_load(f + 2048, __ATOMIC_RELAXED, __HIP_MEMORY_SCOPE_AGENT);
    const u64 v3 = __hip_atomic_load(f + 3072, __ATOMIC_RELAXED, __HIP_MEMORY_SCOPE_AGENT);
    if (__all(v0 == T && v1 == T && v2 == T && v3 == T)) break;
    __builtin_amdgcn_s_sleep(1);
  }
  __builtin_amdgcn_fence(__ATOMIC_ACQUIRE, "wavefront");
}
DI void publish(unsigned char* ws, u32 off, u64 T) {
  __hip_atomic_store((GAS u64*)(ws + off), T, __ATOMIC_RELAXED, __HIP_MEMORY_SCOPE_AGENT);
}
DI void row_stats(const v4u (&r)[5], float& rstd, float& nb) {
  float s0 = 0.f, s1 = 0.f, q0 = 0.f, q1 = 0.f;
#pragma unroll
  for (int m = 0; m < 5; ++m) {
    s0 = dot2(r[m].x, 0x3f803f80u, s0); q0 = dot2(r[m].x, r[m].x, q0);
    s1 = dot2(r[m].y, 0x3f803f80u, s1); q1 = dot2(r[m].y, r[m].y, q1);
    s0 = dot2(r[m].z, 0x3f803f80u, s0); q0 = dot2(r[m].z, r[m].z, q0);
    s1 = dot2(r[m].w, 0x3f803f80u, s1); q1 = dot2(r[m].w, r[m].w, q1);
  }
  const float mean = row16_sum(s0 + s1) * (1.f / 640.f);
  const float var = fmaxf(row16_sum(q0 + q1) * (1.f / 640.f) - mean * mean, 0.f);
  rstd = __builtin_amdgcn_rsqf(var + 1e-5f);
  nb = -mean * rstd;
}

#define STAMP(i) asm volatile("; MARK " #i ::: "memory")
#define STAMPW(i, ww) do {} while (0)

typedef const GAS unsigned char* gptr;
__global__ __launch_bounds__(NTHR, 2) void fused_block(const u16* a_x, const u16* a_wqkv, const u16* a_wo, const u16* a_w1, const u16* a_b1,
                                                       const u16* a_w2, const u16* a_b2, const u16* a_c, const u16* a_wmod, u16* a_out,
                                                       unsigned char* a_ws) {
  extern __shared__ __attribute__((aligned(16))) unsigned char smem[];
  const __attribute__((address_space(4))) u64* ka = (const __attribute__((address_space(4))) u64*)__builtin_amdgcn_kernarg_segment_ptr();
  u64 k0 = ka[0], k1 = ka[1], k2 = ka[2], k3 = ka[3], k4 = ka[4], k5 = ka[5], k6 = ka[6], k7 = ka[7], k8 = ka[8], k9 = ka[9], k10 = ka[10];
  asm volatile("" : "+s"(k0), "+s"(k1), "+s"(k2), "+s"(k3), "+s"(k4), "+s"(k5), "+s"(k6), "+s"(k7), "+s"(k8), "+s"(k9), "+s"(k10));
  const gptr x = (gptr)k0, wqkv = (gptr)k1, wo = (gptr)k2, w1 = (gptr)k3, b1 = (gptr)k4, w2 = (gptr)k5, b2 = (gptr)k6, cvec = (gptr)k7, wmod = (gptr)k8;
  GAS unsigned char* const out = (GAS unsigned char*)k9;
  unsigned char* const ws = (unsigned char*)k10;
  LAS unsigned char* L = (LAS unsigned char*)smem;
  const int tid = threadIdx.x, lane = tid & 63, b = blockIdx.x;
  const int w = __builtin_amdgcn_readfirstlane(tid >> 6);
  const int xcd = b & 7, jj = b >> 3;
  const int p = (xcd >> 1) * 8 + (jj >> 2), g = (xcd & 1) * 4 + (jj & 3);
  const rsrc_t rs = __builtin_amdgcn_make_buffer_rsrc(ws, 0, WS_USED, 0x00020000);
  const int g4 = lane >> 4, t16 = lane & 15;
  const u32 l16 = lane * 16;
  STAMP(0);

  const u64 Tv = __hip_atomic_load((const GAS u64*)(ws + OFF_E), __ATOMIC_RELAXED, __HIP_MEMORY_SCOPE_AGENT) + 1;
  const int id2 = (w - 3) * 256 + b;
  const bool hasjob = w < 3 ? w * 256 + b < 640 : (w < 7 && id2 < 972);
  const int job = w < 3 ? (hasjob ? w * 256 + b : 0) : (id2 < 540 ? 1072 + id2 : (hasjob ? 640 + id2 - 540 : 1072));
  gptr jsrc; u32 jstride, jdst, jA, jB; int ppr; bool paired;
  if (job < 480) {
    const int gg = job / 60, rem = job % 60, part = rem / 20, j = rem % 20;
    jsrc = wqkv + (u32)(32 * j * 3840 + part * 1280 + gg * 160);
    jstride = 3840; ppr = 10; paired = false;
    jdst = OFF_WQ + (gg * 15 + part * 5) * 20480 + j * 1024; jA = 40960; jB = 20480;
  } else if (job < 640) {
    const int id = job - 480, gg = id / 20, j = id % 20;
    jsrc = wo + (u32)(32 * j * 1280 + gg * 160);
    jstride = 1280; ppr = 10; paired = true;
    jdst = OFF_WO + (gg * 20 + j) * 5120; jA = 2048; jB = 1024;
  } else if (job < 1072) {
    const int id = job - 640, gg = id / 54, j = id % 54;
    jsrc = w2 + (u32)(32 * j * 1280 + gg * 160);
    jstride = 1280; ppr = 10; paired = true;
    jdst = OFF_W2 + (gg * 54 + j) * 5120; jA = 2048; jB = 1024;
  } else {
    const int id = job - 1072, jb2 = id / 20, j = id % 20;
    jsrc = w1 + (u32)(32 * j * 3456 + jb2 * 128);
    jstride = 3456; ppr = 8; paired = true;
    jdst = OFF_W1 + (2 * jb2 * 20 + j) * 2048; jA = 40960; jB = 1024;
  }
  const int cl = b % 60, ks = b < 240 ? b / 60 : 3;
  v4u gw[3];
#pragma unroll
  for (int i = 0; i < 3; ++i) {
    const int rr = (lane >> 3) + 8 * i;
    const u32 k = 160 * ks + 20 * w + (rr < 20 ? rr : 0);
    gw[i] = *(const GAS v4u*)(wmod + (k * 7680 + cl * 128 + (lane & 7) * 16));
    SCHED();
  }
  const u16 gc = *(const GAS u16*)(cvec + (160 * ks + 20 * w + (lane < 20 ? lane : 0)) * 2);
  SCHED();
  v4u jr[5];
  if (w >= 3) asm volatile("s_barrier" ::: "memory");
  {
    const u32 magic = ppr == 10 ? 6554u : 8192u;
#pragma unroll
    for (int it = 0; it < 5; ++it) {
      u32 idx = it * 64 + lane;
      if (it == 4 && ppr == 8) idx = lane;
      const u32 row = (idx * magic) >> 16, pc = idx - row * ppr;
      jr[it] = *(const GAS v4u*)(jsrc + (row * jstride + pc * 16));
      SCHED();
    }
  }
  if (w < 3) asm volatile("s_barrier" ::: "memory");
  u32 pf = 0;
  if (w == 7) {
    u64 pc; int len;
    asm volatile("fused_pf_begin:\n\ts_getpc_b64 %0\n\ts_mov_b32 %1, fused_pf_end-fused_pf_begin" : "=s"(pc), "=s"(len));
    const int o = (jj * 16 + t16) * 128;
    if (o < len) pf = *(const GAS u32*)(pc + (u32)o);
  }
  SCHED();
  STAMP(18);
  {
    const float c = lane < 20 ? bf2f(gc) : 0.f;
    const float sl = c * __builtin_amdgcn_rcpf(1.f + __expf(-c));
    f32x2 acc[4];
#pragma unroll
    for (int i = 0; i < 3; ++i) {
      const int rr = (lane >> 3) + 8 * i;
      const float s = __builtin_bit_cast(float, __builtin_amdgcn_ds_bpermute(rr * 4, __builtin_bit_cast(int, sl)));
      const f32x2 s2 = {s, s};
#pragma unroll
      for (int e = 0; e < 4; ++e) acc[e] = i == 0 ? unpk(gw[i][e]) * s2 : fma2(unpk(gw[i][e]), s2, acc[e]);
    }
    float u[8];
#pragma unroll
    for (int e = 0; e < 4; ++e) { u[2 * e] = dpp_add<0x128>(acc[e].x); u[2 * e + 1] = dpp_add<0x128>(acc[e].y); }
    float v[4];
#pragma unroll
    for (int e = 0; e < 4; ++e) v[e] = swap16_add(u[2 * e], u[2 * e + 1]);
    const float z0 = swap32_add(v[0], v[1]), z1 = swap32_add(v[2], v[3]);
    const float z = (lane & 8) ? z1 : z0;
    const int col = (lane & 7) * 8 + ((lane >> 1) & 4) + ((lane >> 4) & 2) + ((lane >> 4) & 1);
    *(LAS float*)(L + L_GV + w * 256 + col * 4) = z;
  }
  STAMPW(22, 0); STAMPW(23, 5); STAMPW(24, 7);
  bar();
  if (b < 240 && w == 0) {
    float s = 0.f;
#pragma unroll
    for (int ww = 0; ww < 8; ++ww) s += *(LAS float*)(L + L_GV + ww * 256 + lane * 4);
    __builtin_amdgcn_raw_buffer_store_b32(__builtin_bit_cast(u32, s), rs, lane * 4, OFF_MODP + (ks * 3840 + cl * 64) * 4, SC1);
  }
  STAMP(19);
  auto run_job = [&]() {
    LAS unsigned char* sc = L + L_JOB + (w < 7 ? w : 0) * 5120;
#pragma unroll
    for (int it = 0; it < 5; ++it)
      if (it < 4 || ppr == 10) *(LAS v4u*)(sc + it * 1024 + l16) = jr[it];
    __builtin_amdgcn_fence(__ATOMIC_RELEASE, "wavefront");
    __builtin_amdgcn_wave_barrier();
    __builtin_amdgcn_fence(__ATOMIC_ACQUIRE, "wavefront");
    const int rowb = ppr * 16;
    const int q = t16 >> 2, pp = lane & 3;
    const int rbase = (8 * g4 + q) * rowb;
#pragma unroll
    for (int ct = 0; ct < 5; ++ct) {
      if (ct < 4 || ppr == 10) {
        const int cb = (paired && ct < 4) ? 64 * (ct >> 1) + 8 * (ct & 1) + 16 * pp : 32 * ct + 8 * pp;
        const v2u lo = ldstr(sc, rbase + cb), hi = ldstr(sc, rbase + 4 * rowb + cb);
        stw(rs, v4u{lo.x, lo.y, hi.x, hi.y}, l16, jdst + (ct >> 1) * jA + (ct & 1) * jB);
      }
    }
  };
  if (hasjob && w < 3) run_job();
  STAMPW(25, 0); STAMPW(26, 5);
  v4u xr[3][5];
#pragma unroll
  for (int a = 0; a < 3; ++a) {
    if (a < 2 || w < 4) {
      int tok = 64 * p - 16 + 4 * (w + 8 * a) + g4;
      tok = tok < 0 ? 0 : tok;
#pragma unroll
      for (int m = 0; m < 5; ++m) { xr[a][m] = *(const GAS v4u*)(x + (u32)(tok * 1280 + (t16 + 16 * m) * 16)); SCHED(); }
    } else {
#pragma unroll
      for (int m = 0; m < 5; ++m) xr[a][m] = v4u{0, 0, 0, 0};
    }
  }
  STAMP(20);
  if (w < 3) VMCNT(15);
  asm volatile("" ::"v"(pf));
  bar();
  const u32 Tlo = __builtin_amdgcn_readfirstlane((u32)Tv), Thi = __builtin_amdgcn_readfirstlane((u32)(Tv >> 32));
  const u64 T = ((u64)Thi << 32) | Tlo;
  if (tid == 0) publish(ws, OFF_F0 + b * 128, T);
  STAMP(1);
  if (hasjob && w >= 3) run_job();
#pragma unroll
  for (int a = 0; a < 3; ++a) {
    if (a < 2 || w < 4) {
      float rstd, nb;
      row_stats(xr[a], rstd, nb);
      const f32x2 r2 = {rstd, rstd}, n2 = {nb, nb};
#pragma unroll
      for (int m = 0; m < 5; ++m) {
#pragma unroll
        for (int e = 0; e < 4; ++e) xr[a][m][e] = pk(fma2(unpk(xr[a][m][e]), r2, n2));
        asm volatile("" : "+v"(xr[a][m]));
      }
    }
  }
  v4u pv1[5][4];
  if (w == 7) {
    poll256(ws, OFF_F0, lane, T);
    if (b == 0 && lane == 0) publish(ws, OFF_E, T);
#pragma unroll
    for (int r = 0; r < 5; ++r)
#pragma unroll
      for (int k = 0; k < 4; ++k) { pv1[r][k] = ldw(rs, (r * 64 + lane) * 16, OFF_MODP + k * 15360); SCHED(); }
  }
  STAMP(3);
  if (w == 7) {
#pragma unroll
    for (int r = 0; r < 5; ++r) {
      const f32x4 s = (__builtin_bit_cast(f32x4, pv1[r][0]) + __builtin_bit_cast(f32x4, pv1[r][1])) + (__builtin_bit_cast(f32x4, pv1[r][2]) + __builtin_bit_cast(f32x4, pv1[r][3]));
      const int qd = r * 64 + lane, col = (qd < 160 ? qd : qd - 160) * 4;
      const int vo = L_VEC + (col >> 3) * 64 + (col & 4) * 4;
      if (qd < 160) *(LAS f32x4*)(L + vo + 32) = s;
      else *(LAS f32x4*)(L + vo) = s + 1.f;
    }
  }
  STAMP(2);
  if (w >= 3 && w < 7) drain();
  bar();
  if (tid == 192) publish(ws, OFF_F0B + b * 128, T);

  constexpr int R1 = 6;
  v4u wr[R1][2];
  const u32 wq0 = OFF_WQ + (g * 15 + 2 * w) * 20480, wq1 = OFF_WQ + (g * 15 + (w < 7 ? 2 * w + 1 : 14)) * 20480;
#pragma unroll
  for (int j = 0; j < R1; ++j) {
    if (j == 2 || j == 4) asm volatile("s_barrier" ::: "memory");
    wr[j][0] = ldw(rs, l16 + (j & 3) * 1024, wq0 + (j >> 2) * 4096); wr[j][1] = ldw(rs, l16 + (j & 3) * 1024, wq1 + (j >> 2) * 4096);
    SCHED();
  }
  f32x4 acc1[5][2];
#pragma unroll
  for (int i = 0; i < 5; ++i) { acc1[i][0] = f32x4{0.f, 0.f, 0.f, 0.f}; acc1[i][1] = f32x4{0.f, 0.f, 0.f, 0.f}; }
  const int rd_even = g4 * 256 + ((t16 ^ g4) * 16), rd_odd = rd_even ^ 64;
  int wad[3];
#pragma unroll
  for (int a = 0; a < 3; ++a) {
    const int grp = w + 8 * a, tt = grp >> 2, t = 4 * (grp & 3) + g4;
    wad[a] = L_A + tt * 20480 + (t16 >> 2) * 1024 + (t16 & 3) * 256 + ((t ^ (t16 & 7)) * 16);
  }
  const int vad = L_VEC + t16 * 64;
  f32x4 cm[2], sv[2];
  auto ldmod = [&](int m, int i) { cm[i] = ldsf4(L, vad + m * 1024 + i * 16); sv[i] = ldsf4(L, vad + m * 1024 + 32 + i * 16); };
  auto modf = [&](f32x2 u, int d) {
    return (d & 1) ? f32x2{fma1(u.x, cm[d >> 1].z, sv[d >> 1].z), fma1(u.y, cm[d >> 1].w, sv[d >> 1].w)}
                   : f32x2{fma1(u.x, cm[d >> 1].x, sv[d >> 1].x), fma1(u.y, cm[d >> 1].y, sv[d >> 1].y)};
  };
  ldmod(0, 0); ldmod(0, 1);
#pragma unroll
  for (int a = 0; a < 3; ++a)
    if (a < 2 || w < 4)
      *(LAS v4u*)(L + wad[a]) = v4u{pk(fma2(unpk(xr[a][0].x), cm[0].xy, sv[0].xy)), pk(fma2(unpk(xr[a][0].y), cm[0].zw, sv[0].zw)),
                                    pk(fma2(unpk(xr[a][0].z), cm[1].xy, sv[1].xy)), pk(fma2(unpk(xr[a][0].w), cm[1].zw, sv[1].zw))};
  ldmod(1, 0); ldmod(1, 1);
  bar();
  STAMP(4);
  v4u pvs[4];
  {
    int rdA[2] = {L_A + rd_even, L_A + rd_odd}, rdB[2] = {L_A + 81920 + rd_even, L_A + 81920 + rd_odd};
    asm volatile("" : "+v"(rdA[0]), "+v"(rdA[1]), "+v"(rdB[0]), "+v"(rdB[1]));
    auto rd = [&](int tt, int j) { return tt < 4 ? lds128(L, rdA[j & 1] + tt * 20480 + j * 1024) : lds128(L, rdB[j & 1] + j * 1024); };
    v4u af[2][5];
#pragma unroll
    for (int tt = 0; tt < 5; ++tt) af[0][tt] = rd(tt, 0);
    SCHED();
    auto kstep = [&](auto jc, auto prepc) {
      constexpr int j = decltype(jc)::value;
      constexpr bool prep = decltype(prepc)::value;
      constexpr int m = j >> 2, q = j & 3, cu = j & 1, nx = cu ^ 1, mp = m + 1 < 5 ? m + 1 : 4, qa = q < 3 ? q : 0;
      constexpr bool more = j + 1 < 20 && !ABL_NORD1, fill = j + R1 < 20 && !ABL_NOFILL1, nmod = m < 3 && q == 3 && !ABL_NOPREP;
      if (ABL_NORD1 && j + 1 < 20) { for (int tt = 0; tt < 5; ++tt) af[nx][tt] = af[cu][tt]; }
      const v4u wa = wr[j % R1][0], wb = wr[j % R1][1];
      const v4u xv = xr[qa][mp];
      float a0, a1, b0, b1v_, c0, c1, d0, d1; v4u o;
      acc1[0][0] = mfma(wa, af[cu][0], acc1[0][0]);
      if (more) af[nx][0] = rd(0, j + 1);
      if (prep) { a0 = blo(xv.x); a1 = bhi(xv.x); }
      SCHED();
      acc1[0][1] = mfma(wb, af[cu][0], acc1[0][1]);
      if (prep) { b0 = blo(xv.y); b1v_ = bhi(xv.y); }
      if (nmod) ldmod(m + 2 < 5 ? m + 2 : 4, 0);
      SCHED();
      acc1[1][0] = mfma(wa, af[cu][1], acc1[1][0]);
      if (more) af[nx][1] = rd(1, j + 1);
      if (prep) { a0 = fma1(a0, cm[0].x, sv[0].x); a1 = fma1(a1, cm[0].y, sv[0].y); }
      SCHED();
      acc1[1][1] = mfma(wb, af[cu][1], acc1[1][1]);
      if (fill) wr[j % R1][0] = ldw(rs, l16 + ((j + R1) & 3) * 1024, wq0 + ((j + R1) >> 2) * 4096);
      if (prep) { c0 = blo(xv.z); c1 = bhi(xv.z); }
      SCHED();
      acc1[2][0] = mfma(wa, af[cu][2], acc1[2][0]);
      if (more) af[nx][2] = rd(2, j + 1);
      if (prep) { b0 = fma1(b0, cm[0].z, sv[0].z); b1v_ = fma1(b1v_, cm[0].w, sv[0].w); }
      SCHED();
      acc1[2][1] = mfma(wb, af[cu][2], acc1[2][1]);
      if (prep) { o.x = pk(f32x2{a0, a1}); d0 = blo(xv.w); }
      if (nmod) ldmod(m + 2 < 5 ? m + 2 : 4, 1);
      SCHED();
      acc1[3][0] = mfma(wa, af[cu][3], acc1[3][0]);
      if (more) af[nx][3] = rd(3, j + 1);
      if (prep) { d1 = bhi(xv.w); c0 = fma1(c0, cm[1].x, sv[1].x); }
      SCHED();
      acc1[3][1] = mfma(wb, af[cu][3], acc1[3][1]);
      if (fill) wr[j % R1][1] = ldw(rs, l16 + ((j + R1) & 3) * 1024, wq1 + ((j + R1) >> 2) * 4096);
      if (prep) { c1 = fma1(c1, cm[1].y, sv[1].y); o.y = pk(f32x2{b0, b1v_}); }
      SCHED();
      acc1[4][0] = mfma(wa, af[cu][4], acc1[4][0]);
      if (more) af[nx][4] = rd(4, j + 1);
      if (prep) { d0 = fma1(d0, cm[1].z, sv[1].z); d1 = fma1(d1, cm[1].w, sv[1].w); }
      SCHED();
      acc1[4][1] = mfma(wb, af[cu][4], acc1[4][1]);
      if (prep) {
        o.z = pk(f32x2{c0, c1}); o.w = pk(f32x2{d0, d1});
        if (ABL_P == 1) asm volatile("" :: "v"(o));
        else if (ABL_P == 2) *(LAS v4u*)(L + wad[qa] + mp * 4096) = xv;
        else *(LAS v4u*)(L + wad[qa] + mp * 4096) = o;
      }
      SCHED();
    };
    static_for<20>([&](auto jc) {
      constexpr int j = decltype(jc)::value, m = j >> 2, q = j & 3;
      if constexpr (j == 16) {
        const u32 vo = 1920 * 4 + (tid < 320 ? tid : 0) * 16;
#pragma unroll
        for (int k = 0; k < 4; ++k) { pvs[k] = ldw(rs, vo, OFF_MODP + k * 15360); SCHED(); }
      }
      if constexpr (m < 4 && q < 2 && !ABL_NOPREP) kstep(jc, BoolC<true>{});
      else if constexpr (m < 4 && q == 2 && !ABL_NOPREP) { if (w < 4) kstep(jc, BoolC<true>{}); else kstep(jc, BoolC<false>{}); }
      else kstep(jc, BoolC<false>{});
      if constexpr (m < 4 && q == 2 && !ABL_NOBAR1) bar();
    });
  }
  bar();
  STAMP(5);
  if (tid < 320) {
    f32x4 s = (__builtin_bit_cast(f32x4, pvs[0]) + __builtin_bit_cast(f32x4, pvs[1])) + (__builtin_bit_cast(f32x4, pvs[2]) + __builtin_bit_cast(f32x4, pvs[3]));
    if (tid >= 160) s += 1.f;
    *(LAS v2u*)(L + L_SPK + tid * 8) = v2u{pk(s.xy), pk(s.zw)};
  } else if (tid < 324) {
    *(LAS u32*)(L + L_ONE + (tid - 320) * 4) = 0x3f803f80u;
    *(LAS u32*)(L + L_ONE + 16 + (tid - 320) * 4) = 0u;
  }
#pragma unroll
  for (int i = 0; i < 4; ++i) {
    const int gi = w + 8 * i;
    if (gi < 25) dma<4>(rs, l16, OFF_WO + g * 102400 + gi * 4096, L_WO + gi * 4096);
  }
#pragma unroll
  for (int tt = 0; tt < 5; ++tt)
#pragma unroll
    for (int c = 0; c < 2; ++c)
      if (c == 0 || w < 7)
        *(LAS v2u*)(L + L_A + (tt * 16 + t16) * QS + (2 * w + c) * 32 + g4 * 8) =
            v2u{pack2(acc1[tt][c].x, acc1[tt][c].y), pack2(acc1[tt][c].z, acc1[tt][c].w)};
  bar();
  STAMP(6);
  {
    const int h = w & 3, par = w >> 2;
    const float cs = 0.22360679774997896f * 1.4426950408889634f;
    const int vq = t16 >> 2, vp = lane & 3;
    v4u qf[2], kf[2][2], vf0[2], vf1[2];
#pragma unroll
    for (int qt = 0; qt < 2; ++qt) {
      const int qo = L_A + (2 * (8 + 16 * qt + t16) + par) * QS + 40 * h + (g4 < 3 ? 16 * g4 : 0);
      const v2u qlo = lds64(L, qo), qhi = lds64(L, qo + 8);
      qf[qt] = v4u{g4 == 3 ? 0u : qlo.x, g4 == 3 ? 0u : qlo.y, g4 >= 2 ? 0u : qhi.x, g4 >= 2 ? 0u : qhi.y};
#pragma unroll
      for (int kt = 0; kt < 2; ++kt) {
        int ki = 16 * qt + 16 * kt + t16;
        ki = ki > 39 ? 39 : ki;
        const int ko = L_A + (2 * ki + par) * QS + 160 + 40 * h + 16 * g4;
        const v2u klo = lds64(L, ko), khi = lds64(L, ko + 8);
        kf[qt][kt] = v4u{klo.x, klo.y, khi.x, khi.y};
      }
      int ka2 = 16 * qt + 4 * g4 + vq, kb = ka2 + 16;
      kb = kb > 39 ? 39 : kb;
      const int va = L_A + (2 * ka2 + par) * QS + 320 + 40 * h, vb = L_A + (2 * kb + par) * QS + 320 + 40 * h;
      const v2u a0 = ldstr(L, va + 8 * vp), b0 = ldstr(L, vb + 8 * vp), a1 = ldstr(L, va + 32), b1x = ldstr(L, vb + 32);
      vf0[qt] = v4u{a0.x, a0.y, b0.x, b0.y}; vf1[qt] = v4u{a1.x, a1.y, b1x.x, b1x.y};
    }
    f32x4 sc[2][2];
#pragma unroll
    for (int qt = 0; qt < 2; ++qt)
#pragma unroll
      for (int kt = 0; kt < 2; ++kt) sc[qt][kt] = mfma(kf[qt][kt], qf[qt], f32x4{0.f, 0.f, 0.f, 0.f});
    const float NEG = -3.0e38f;
#pragma unroll
    for (int kt = 0; kt < 2; ++kt)
#pragma unroll
      for (int r = 0; r < 4; ++r) {
        const bool ok = (unsigned)(16 * kt + 4 * g4 + r - t16 - 1) < 8u;
        sc[0][kt][r] = ok ? sc[0][kt][r] : NEG;
        sc[1][kt][r] = ok ? sc[1][kt][r] : NEG;
      }
    if (p == 0) {
#pragma unroll
      for (int r = 0; r < 4; ++r) sc[0][0][r] = g4 >= 2 ? sc[0][0][r] : NEG;
    }
    float mx[2], sum[2];
#pragma unroll
    for (int qt = 0; qt < 2; ++qt)
      mx[qt] = fmaxf(fmaxf(fmaxf(sc[qt][0].x, sc[qt][0].y), fmaxf(sc[qt][0].z, sc[qt][0].w)), fmaxf(fmaxf(sc[qt][1].x, sc[qt][1].y), fmaxf(sc[qt][1].z, sc[qt][1].w)));
#pragma unroll
    for (int qt = 0; qt < 2; ++qt) mx[qt] = x16_max(mx[qt]);
#pragma unroll
    for (int qt = 0; qt < 2; ++qt) mx[qt] = x32_max(mx[qt]) * cs;
#pragma unroll
    for (int qt = 0; qt < 2; ++qt) {
#pragma unroll
      for (int kt = 0; kt < 2; ++kt)
#pragma unroll
        for (int r = 0; r < 4; ++r) sc[qt][kt][r] = __builtin_amdgcn_exp2f(sc[qt][kt][r] * cs - mx[qt]);
      const f32x4 t4 = sc[qt][0] + sc[qt][1];
      sum[qt] = (t4.x + t4.y) + (t4.z + t4.w);
    }
#pragma unroll
    for (int qt = 0; qt < 2; ++qt) sum[qt] = x16_sum(sum[qt]);
#pragma unroll
    for (int qt = 0; qt < 2; ++qt) sum[qt] = __builtin_amdgcn_rcpf(x32_sum(sum[qt]));
#pragma unroll
    for (int qt = 0; qt < 2; ++qt) {
      const f32x2 iv = {sum[qt], sum[qt]};
      const v4u pf4 = {pk(sc[qt][0].xy * iv), pk(sc[qt][0].zw * iv), pk(sc[qt][1].xy * iv), pk(sc[qt][1].zw * iv)};
      const f32x4 o0 = mfma(vf0[qt], pf4, f32x4{0.f, 0.f, 0.f, 0.f});
      const f32x4 o1 = mfma(vf1[qt], pf4, f32x4{0.f, 0.f, 0.f, 0.f});
      const int orow = L_OB + (par * 32 + 16 * qt + t16) * OS + 40 * h;
      *(LAS v2u*)(L + orow + 8 * g4) = v2u{pk(o0.xy), pk(o0.zw)};
      if (g4 == 0) *(LAS v2u*)(L + orow + 32) = v2u{pk(o1.xy), pk(o1.zw)};
    }
  }
  bar();
  STAMP(21);
  {
    const int tok = 2 * (lane & 31) + (lane >> 5);
#pragma unroll
    for (int i = 0; i < 2; ++i) {
      const int u = w + 8 * i;
      if (u < 10) {
        const int ch = 10 * g + u;
        const v4u v = lds128(L, L_OB + lane * OS + 16 * u);
        stw(rs, v, (tok >> 4) * 20480 + (tok & 15) * 16, OFF_O + p * 81920 + (ch >> 2) * 1024 + (ch & 3) * 256);
      }
    }
  }
  drain();
  bar();
  if (tid == 0) publish(ws, OFF_F1 + (p * 8 + g) * 128, T);
  STAMP(7);

  const int s4 = w & 3, kh = w >> 2;
  const int tokg = 64 * p + 16 * s4 + t16;
  const int fo = kh == 0 ? 8 * g4 : 32 + 8 * g4;
  const v4u xa = *(const GAS v4u*)(x + (u32)(tokg * 1280 + (80 * g + fo) * 2));
  const v2u xb = *(const GAS v2u*)(x + (u32)(tokg * 1280 + (80 * g + 64 + 4 * g4) * 2));
  v4u pv[4]; v2u b2r = {0, 0};
  if (tid < 40) {
    const u32 vo = (tid >= 20 ? 3200 * 4 + (tid - 20) * 16 : 1280 * 4 + tid * 16) + g * 320;
#pragma unroll
    for (int k = 0; k < 4; ++k) pv[k] = ldw(rs, vo, OFF_MODP + k * 15360);
  }
  if (tid >= 64 && tid < 84) b2r = *(const GAS v2u*)(b2 + (80 * g + 4 * (tid - 64)) * 2);
  if (tid >= 128 && tid < 148) {
    const v2u cp = lds64(L, L_CPK + (80 * g + 4 * (tid - 128)) * 2);
    *(LAS f32x4*)(L + L_CF + (tid - 128) * 16) = f32x4{blo(cp.x), bhi(cp.x), blo(cp.y), bhi(cp.y)};
  }
  if (w == 6) {
    if (b < 108) {
      poll256(ws, OFF_F0B, lane, T);
      v4u uw[20];
#pragma unroll
      for (int j = 0; j < 20; ++j) { uw[j] = ldw(rs, l16 + (j & 1) * 2048, OFF_W1 + (b >> 1) * 40960 + (b & 1) * 1024 + (j >> 1) * 4096); SCHED(); }
      int ua = t16 < 2 ? (t16 == 0 ? L_CPK : L_SPK) + g4 * 16 : L_ONE + (t16 == 2 ? 0 : 16);
      const int ui = t16 < 2 ? 64 : 0;
      f32x4 ud = {0.f, 0.f, 0.f, 0.f};
#pragma unroll
      for (int j = 0; j < 20; ++j) { ud = mfma(uw[j], lds128(L, ua), ud); ua += ui; }
      if (t16 < 3) stw(rs, __builtin_bit_cast(v4u, ud), (((b >> 1) * 3 + t16) * 32 + 8 * g4 + 4 * (b & 1)) * 4, OFF_UVZ);
    }
  } else if (w == 7) {
    poll256(ws, OFF_F0B, lane, T);
    poll(ws, OFF_F1 + p * 8 * 128, 8, lane, T);
  }
  if (tid < 40) {
    f32x4 s = {0.f, 0.f, 0.f, 0.f};
#pragma unroll
    for (int k = 0; k < 4; ++k) s += __builtin_bit_cast(f32x4, pv[k]);
    *(LAS f32x4*)(L + L_SM + tid * 16) = s;
  }
  if (tid >= 64 && tid < 84) *(LAS f32x4*)(L + L_SM + 640 + (tid - 64) * 16) = f32x4{blo(b2r.x), bhi(b2r.x), blo(b2r.y), bhi(b2r.y)};
  bar();
  STAMP(8);
  v4u oa[10];
#pragma unroll
  for (int i = 0; i < 4; ++i) { oa[i] = ldw(rs, l16 + (i & 3) * 1024, OFF_O + (p * 4 + s4) * 20480 + kh * 10240 + (i >> 2) * 4096); SCHED(); }
  SCHED();
  f32x4 acc2[5];
#pragma unroll
  for (int i = 0; i < 5; ++i) acc2[i] = f32x4{0.f, 0.f, 0.f, 0.f};
  {
    v4u wf[2][5];
    const int wb2 = L_WO + kh * 51200 + l16;
#pragma unroll
    for (int ct = 0; ct < 5; ++ct) wf[0][ct] = lds128(L, wb2 + ct * 1024);
    SCHED();
#pragma unroll
    for (int i = 0; i < 10; ++i) {
#pragma unroll
      for (int ct = 0; ct < 5; ++ct) {
        acc2[ct] = mfma(wf[i & 1][ct], oa[i], acc2[ct]);
        if (i + 1 < 10) wf[(i + 1) & 1][ct] = lds128(L, wb2 + ((i + 1) * 5 + ct) * 1024);
        if (ct == 1 && i + 4 < 10) oa[i + 4] = ldw(rs, l16 + ((i + 4) & 3) * 1024, OFF_O + (p * 4 + s4) * 20480 + kh * 10240 + ((i + 4) >> 2) * 4096);
        SCHED();
      }
    }
  }
  if (kh == 0) {
    *(LAS f32x4*)(L + L_RED + (s4 * 5 + 2) * 1024 + l16) = acc2[2];
    *(LAS f32x4*)(L + L_RED + (s4 * 5 + 3) * 1024 + l16) = acc2[3];
  } else {
    *(LAS f32x4*)(L + L_RED + (s4 * 5 + 0) * 1024 + l16) = acc2[0];
    *(LAS f32x4*)(L + L_RED + (s4 * 5 + 1) * 1024 + l16) = acc2[1];
    *(LAS f32x4*)(L + L_RED + (s4 * 5 + 4) * 1024 + l16) = acc2[4];
  }
  bar();
  STAMP(9);
  v4u x1a; v2u x1b = {0, 0};
  {
    const int c0 = kh == 0 ? 0 : 2;
    const f32x4 u0 = (kh == 0 ? acc2[0] : acc2[2]) + ldsf4(L, L_RED + (s4 * 5 + c0) * 1024 + l16);
    const f32x4 u1 = (kh == 0 ? acc2[1] : acc2[3]) + ldsf4(L, L_RED + (s4 * 5 + c0 + 1) * 1024 + l16);
    const f32x4 g0 = ldsf4(L, L_SM + fo * 4), g1 = ldsf4(L, L_SM + fo * 4 + 16);
    x1a.x = pk(fma2(g0.xy, u0.xy, unpk(xa.x)));
    x1a.y = pk(fma2(g0.zw, u0.zw, unpk(xa.y)));
    x1a.z = pk(fma2(g1.xy, u1.xy, unpk(xa.z)));
    x1a.w = pk(fma2(g1.zw, u1.zw, unpk(xa.w)));
    const u32 xco = OFF_XC + (p * 4 + s4) * 20480 + t16 * 16;
    const f32x4 cf0 = ldsf4(L, L_CF + fo * 4), cf1 = ldsf4(L, L_CF + fo * 4 + 16);
    const v4u cpk = lds128(L, L_CPK + (80 * g + fo) * 2), spk = lds128(L, L_SPK + (80 * g + fo) * 2);
    const v4u xc = {pk(unpk(x1a.x) * cf0.xy), pk(unpk(x1a.y) * cf0.zw), pk(unpk(x1a.z) * cf1.xy), pk(unpk(x1a.w) * cf1.zw)};
    { const int ch = 10 * g + (fo >> 3); stw(rs, xc, (ch >> 2) * 1024 + (ch & 3) * 256, xco); }
    const u32 one2 = 0x3f803f80u;
    float S1 = 0.f, S2 = 0.f, T1 = 0.f, T2 = 0.f, T3 = 0.f, T4 = 0.f;
#pragma unroll
    for (int e = 0; e < 4; ++e) {
      S1 = dot2(x1a[e], one2, S1); S2 = dot2(x1a[e], x1a[e], S2); T1 = dot2(xc[e], one2, T1);
      T2 = dot2(xc[e], xc[e], T2); T3 = dot2(xc[e], cpk[e], T3); T4 = dot2(xc[e], spk[e], T4);
    }
    if (kh == 0) {
      const f32x4 u2 = acc2[4] + ldsf4(L, L_RED + (s4 * 5 + 4) * 1024 + l16);
      const f32x4 g2 = ldsf4(L, L_SM + (64 + 4 * g4) * 4);
      x1b.x = pk(fma2(g2.xy, u2.xy, unpk(xb.x)));
      x1b.y = pk(fma2(g2.zw, u2.zw, unpk(xb.y)));
      const f32x4 cf2 = ldsf4(L, L_CF + (64 + 4 * g4) * 4);
      const v2u cp2 = lds64(L, L_CPK + (80 * g + 64 + 4 * g4) * 2), sp2 = lds64(L, L_SPK + (80 * g + 64 + 4 * g4) * 2);
      const v2u xc2 = {pk(unpk(x1b.x) * cf2.xy), pk(unpk(x1b.y) * cf2.zw)};
      const int ch = 10 * g + 8 + (g4 >> 1);
      __builtin_amdgcn_raw_buffer_store_b64(xc2, rs, xco + (ch >> 2) * 1024 + (ch & 3) * 256 + (g4 & 1) * 8, 0, SC1);
#pragma unroll
      for (int e = 0; e < 2; ++e) {
        S1 = dot2(x1b[e], one2, S1); S2 = dot2(x1b[e], x1b[e], S2); T1 = dot2(xc2[e], one2, T1);
        T2 = dot2(xc2[e], xc2[e], T2); T3 = dot2(xc2[e], cp2[e], T3); T4 = dot2(xc2[e], sp2[e], T4);
      }
    }
    const float m0 = x32_sum(swap16_add(S1, S2)), m1 = x32_sum(swap16_add(T1, T2)), m2 = x32_sum(swap16_add(T3, T4));
    if (g4 < 2) stw(rs, __builtin_bit_cast(v4u, f32x4{m0, m1, m2, 0.f}), ((2 * g + kh) * 2 + g4) * 1024 + (16 * s4 + t16) * 16, OFF_MOM + p * 32768);
  }
  drain();
  bar();
  if (tid == 0) publish(ws, OFF_F2 + (p * 8 + g) * 128, T);
  if (tid == 64 && b < 108) publish(ws, OFF_FU + b * 128, T);
  STAMP(10);

  const int j0 = g < 7 ? 7 * g : 48, nk = g < 6 ? 7 : 6;
  const bool act3 = w < nk;
  const int jb = j0 + (act3 ? w : 0);
  constexpr int R3 = 6;
  v4u w3[R3][2];
  const u32 w1o = OFF_W1 + jb * 40960;
  v4u b1v = {0, 0, 0, 0};
  if (act3) {
#pragma unroll
    for (int j = 0; j < R3; ++j) {
      w3[j][0] = ldw(rs, l16 + (j & 1) * 2048, w1o + (j >> 1) * 4096); w3[j][1] = ldw(rs, l16 + (j & 1) * 2048 + 1024, w1o + (j >> 1) * 4096);
      SCHED();
      if (j == 2) VMCNT(4);
    }
    b1v = *(const GAS v4u*)(b1 + (32 * jb + 8 * g4) * 2);
  }
  SCHED();
  if (w == 7) poll(ws, OFF_F2 + p * 8 * 128, 8, lane, T);
  bar();
  STAMP(11);
#pragma unroll
  for (int i = 0; i < 3; ++i) {
    const int gi = w + 8 * i;
    if (gi < 20) dma<4>(rs, l16, OFF_XC + p * 81920 + gi * 4096, L_A + gi * 4096);
  }
  VMCNT(0);
  bar();
  STAMP(12);
  const u32 w2g = OFF_W2 + g * 54 * 5120;
  f32x4 acc3[4][2];
#pragma unroll
  for (int i = 0; i < 4; ++i) { acc3[i][0] = f32x4{0.f, 0.f, 0.f, 0.f}; acc3[i][1] = f32x4{0.f, 0.f, 0.f, 0.f}; }
  if (act3) {
    const int rdA = L_A + l16;
    auto rd = [&](int tt, int j) { return lds128(L, rdA + tt * 20480 + j * 1024); };
    v4u af[2][4];
#pragma unroll
    for (int tt = 0; tt < 4; ++tt) af[0][tt] = rd(tt, 0);
    SCHED();
#pragma unroll
    for (int j = 0; j < 20; ++j) {
      const int cu = j & 1, nx = cu ^ 1;
      const v4u wa = w3[j % R3][0], wb = w3[j % R3][1];
#pragma unroll
      for (int tt = 0; tt < 4; ++tt) {
        acc3[tt][0] = mfma(wa, af[cu][tt], acc3[tt][0]);
        if (j + 1 < 20) af[nx][tt] = rd(tt, j + 1);
        SCHED();
        acc3[tt][1] = mfma(wb, af[cu][tt], acc3[tt][1]);
        if (j + R3 < 20 && tt == 0 && !ABL_NOFILL3) w3[j % R3][0] = ldw(rs, l16 + ((j + R3) & 1) * 2048, w1o + ((j + R3) >> 1) * 4096);
        if (j + R3 < 20 && tt == 2 && !ABL_NOFILL3) w3[j % R3][1] = ldw(rs, l16 + ((j + R3) & 1) * 2048 + 1024, w1o + ((j + R3) >> 1) * 4096);
        SCHED();
      }
    }
  } else if (w == 7) {
    v4u uv[3], mo[32];
#pragma unroll
    for (int i = 0; i < 32; ++i) { mo[i] = ldw(rs, l16 + (i & 3) * 1024, OFF_MOM + p * 32768 + (i >> 2) * 4096); SCHED(); }
    poll(ws, OFF_FU + 2 * j0 * 128, 2 * nk, lane, T);
#pragma unroll
    for (int i = 0; i < 3; ++i) { uv[i] = ldw(rs, (i * 64 + lane < 24 * nk ? i * 64 + lane : 0) * 16, OFF_UVZ + j0 * 384); SCHED(); }
    float q5[5] = {0.f, 0.f, 0.f, 0.f, 0.f};
    {
      const u32 one2 = 0x3f803f80u;
      const int lo = (lane < 40 ? lane : 0) * 32;
#pragma unroll
      for (int hh = 0; hh < 2; ++hh) {
        const v4u cp = lds128(L, L_CPK + lo + hh * 16), sp = lds128(L, L_SPK + lo + hh * 16);
#pragma unroll
        for (int e = 0; e < 4; ++e) {
          q5[0] = dot2(cp[e], one2, q5[0]); q5[1] = dot2(cp[e], cp[e], q5[1]); q5[2] = dot2(cp[e], sp[e], q5[2]);
          q5[3] = dot2(sp[e], one2, q5[3]); q5[4] = dot2(sp[e], sp[e], q5[4]);
        }
      }
#pragma unroll
      for (int k = 0; k < 5; ++k) q5[k] = x32_sum(x16_sum(row16_sum(lane < 40 ? q5[k] : 0.f)));
    }
    f32x4 ma = {0.f, 0.f, 0.f, 0.f}, mb = {0.f, 0.f, 0.f, 0.f};
#pragma unroll
    for (int i = 0; i < 32; i += 2) { ma += __builtin_bit_cast(f32x4, mo[i]); mb += __builtin_bit_cast(f32x4, mo[i + 1]); }
    const float S1 = ma.x, T1 = ma.y, T3 = ma.z, S2 = mb.x, T2 = mb.y, T4 = mb.z;
    const float iD = 1.f / 640.f;
    const float mean = S1 * iD;
    const float r1 = __builtin_amdgcn_rsqf(fmaxf(S2 * iD - mean * mean, 0.f) + 1e-5f);
    const float H1 = r1 * (T1 - mean * q5[0]) + q5[3];
    const float H2 = r1 * r1 * (T2 - 2.f * mean * T3 + mean * mean * q5[1]) + 2.f * r1 * (T4 - mean * q5[2]) + q5[4];
    const float m2 = H1 * iD;
    const float r2 = __builtin_amdgcn_rsqf(fmaxf(H2 * iD - m2 * m2, 0.f) + 1e-5f);
    const float al = r1 * r2;
    *(LAS f32x4*)(L + L_ROWC + lane * 16) = f32x4{al, -mean * al, r2, -m2 * r2};
#pragma unroll
    for (int i = 0; i < 3; ++i) if (i * 64 + lane < 168) *(LAS v4u*)(L + L_UVZ + (i * 64 + lane) * 16) = uv[i];
    STAMPW(28, 7);
  }
  STAMPW(29, 0);
  bar();
  STAMP(13);
  if (act3) {
    const f32x2 bb[4] = {unpk(b1v.x), unpk(b1v.y), unpk(b1v.z), unpk(b1v.w)};
    const int uo = L_UVZ + w * 384 + g4 * 32;
    const f32x4 u0 = ldsf4(L, uo), u1 = ldsf4(L, uo + 16), v0 = ldsf4(L, uo + 128), v1 = ldsf4(L, uo + 144), z0 = ldsf4(L, uo + 256), z1 = ldsf4(L, uo + 272);
    const f32x2 uu[4] = {u0.xy, u0.zw, u1.xy, u1.zw}, vv[4] = {v0.xy, v0.zw, v1.xy, v1.zw}, zz[4] = {z0.xy, z0.zw, z1.xy, z1.zw};
    const float k1c = -2.f * 0.7978845608028654f * 1.4426950408889634f, k3c = k1c * 0.044715f;
#pragma unroll
    for (int tt = 0; tt < 4; ++tt) {
      const f32x4 rc = ldsf4(L, L_ROWC + (16 * tt + t16) * 16);
      const f32x2 al = {rc.x, rc.x}, be = {rc.y, rc.y}, r2 = {rc.z, rc.z}, ga2 = {rc.w, rc.w};
      v4u o;
#pragma unroll
      for (int e = 0; e < 4; ++e) {
        const f32x2 a2 = (e & 1) ? acc3[tt][e >> 1].zw : acc3[tt][e >> 1].xy;
        const f32x2 v = fma2(al, a2, fma2(be, uu[e], fma2(r2, vv[e], fma2(ga2, zz[e], bb[e]))));
        const f32x2 z = v * fma2(v * v, f32x2{k3c, k3c}, f32x2{k1c, k1c});
        const f32x2 d = f32x2{__builtin_amdgcn_exp2f(z.x), __builtin_amdgcn_exp2f(z.y)} + 1.f;
        o[e] = pk(v * f32x2{__builtin_amdgcn_rcpf(d.x), __builtin_amdgcn_rcpf(d.y)});
      }
      stw(rs, o, l16, OFF_HH + ((p * 4 + tt) * 54 + jb) * 1024);
    }
  } else if (w == 7) {
#pragma unroll 1
    for (int i = 0; i < 18; ++i) dma<4>(rs, l16, w2g + 4 * i * 1024, i * 4096);
    dma<3>(rs, l16, w2g + 72 * 1024, 72 * 1024);
    dma<4>(rs, l16, w2g + 27 * 5 * 1024, 76800);
    dma<1>(rs, l16, w2g + (27 * 5 + 4) * 1024, 76800 + 4096);
  }
  if (act3) drain();
  bar();
  if (tid == 0) publish(ws, OFF_F3 + (p * 8 + g) * 128, T);
  STAMP(14);

  const u32 w2o = w2g + 27 * kh * 5120;
  const u32 ring = kh * 76800;
  if (w < 7) {
#pragma unroll
    for (int i = 0; i < 3; ++i) {
      const int gi = w + 7 * i;
      if (gi < 17) dma<4>(rs, l16, w2g + (27 * 5 + 5 + 4 * gi) * 1024, 81920 + gi * 4096);
      else if (gi == 17) dma<2>(rs, l16, w2g + (27 * 5 + 5 + 68) * 1024, 81920 + 68 * 1024);
      if (i == 1) VMCNT(4);
    }
    VMCNT(0);
  } else {
    poll(ws, OFF_F3 + p * 8 * 128, 8, lane, T);
  }
  bar();
  STAMP(15);
  constexpr int RA4 = 15;
  v4u ha[27];
  const u32 hho = OFF_HH + ((p * 4 + s4) * 54 + 27 * kh) * 1024;
#pragma unroll
  for (int i = 0; i < RA4; ++i) {
    if (i > 0 && i % 3 == 0) asm volatile("s_barrier" ::: "memory");
    ha[i] = ldw(rs, l16 + (i & 3) * 1024, hho + (i >> 2) * 4096); SCHED();
  }
  SCHED();
  f32x4 acc4[5];
#pragma unroll
  for (int i = 0; i < 5; ++i) acc4[i] = f32x4{0.f, 0.f, 0.f, 0.f};
  {
    v4u wf[2][5];
    const int rb0 = ring + l16, rb1 = ring + 61440 + l16;
#pragma unroll
    for (int ct = 0; ct < 5; ++ct) wf[0][ct] = lds128(L, rb0 + ct * 1024);
    SCHED();
#pragma unroll
    for (int i = 0; i < 27; ++i) {
      const int n = i / 3;
#pragma unroll
      for (int ct = 0; ct < 5; ++ct) {
        acc4[ct] = mfma(wf[i & 1][ct], ha[i], acc4[ct]);
        if (i + 1 < 27) wf[(i + 1) & 1][ct] = lds128(L, (((i + 1) % 15) < 12 ? rb0 : rb1) + (((i + 1) % 15) % 12 * 5 + ct) * 1024);
        if (ct == 1 && i + RA4 < 27) ha[i + RA4] = ldw(rs, l16 + ((i + RA4) & 3) * 1024, hho + ((i + RA4) >> 2) * 4096);
        SCHED();
      }
      if (i % 3 == 2) {
        if (n == 3) VMCNT(15);
        if (n == 4) VMCNT(12);
        if (n == 5) VMCNT(6);
        if (n == 6) VMCNT(0);
        bar();
        if (n < 4 && !ABL_NOREFILL4) {
          if (s4 < 3) dma<4>(rs, l16, w2o + (75 + 15 * n + 4 * s4) * 1024, ring + (15 * n + 4 * s4) * 1024);
          else dma<3>(rs, l16, w2o + (75 + 15 * n + 12) * 1024, ring + (15 * n + 12) * 1024);
        }
      }
    }
  }
  STAMP(16);
  if (kh == 0) {
    *(LAS f32x4*)(L + L_RED4 + (s4 * 5 + 2) * 1024 + l16) = acc4[2];
    *(LAS f32x4*)(L + L_RED4 + (s4 * 5 + 3) * 1024 + l16) = acc4[3];
  } else {
    *(LAS f32x4*)(L + L_RED4 + (s4 * 5 + 0) * 1024 + l16) = acc4[0];
    *(LAS f32x4*)(L + L_RED4 + (s4 * 5 + 1) * 1024 + l16) = acc4[1];
    *(LAS f32x4*)(L + L_RED4 + (s4 * 5 + 4) * 1024 + l16) = acc4[4];
  }
  bar();
  {
    const int c0 = kh == 0 ? 0 : 2;
    const f32x4 u0 = (kh == 0 ? acc4[0] : acc4[2]) + ldsf4(L, L_RED4 + (s4 * 5 + c0) * 1024 + l16);
    const f32x4 u1 = (kh == 0 ? acc4[1] : acc4[3]) + ldsf4(L, L_RED4 + (s4 * 5 + c0 + 1) * 1024 + l16);
    const f32x4 g0 = ldsf4(L, L_SM + 320 + fo * 4), g1 = ldsf4(L, L_SM + 320 + fo * 4 + 16);
    const f32x4 e0 = ldsf4(L, L_SM + 640 + fo * 4), e1 = ldsf4(L, L_SM + 640 + fo * 4 + 16);
    v4u o;
    o.x = pk(fma2(g0.xy, u0.xy + e0.xy, unpk(x1a.x)));
    o.y = pk(fma2(g0.zw, u0.zw + e0.zw, unpk(x1a.y)));
    o.z = pk(fma2(g1.xy, u1.xy + e1.xy, unpk(x1a.z)));
    o.w = pk(fma2(g1.zw, u1.zw + e1.zw, unpk(x1a.w)));
    *(GAS v4u*)(out + (u32)(tokg * 1280 + (80 * g + fo) * 2)) = o;
    if (kh == 0) {
      const f32x4 u2 = acc4[4] + ldsf4(L, L_RED4 + (s4 * 5 + 4) * 1024 + l16);
      const f32x4 g2 = ldsf4(L, L_SM + 320 + (64 + 4 * g4) * 4), e2 = ldsf4(L, L_SM + 640 + (64 + 4 * g4) * 4);
      const v2u o2 = {pk(fma2(g2.xy, u2.xy + e2.xy, unpk(x1b.x))), pk(fma2(g2.zw, u2.zw + e2.zw, unpk(x1b.y)))};
      *(GAS v2u*)(out + (u32)(tokg * 1280 + (80 * g + 64 + 4 * g4) * 2)) = o2;
    }
  }
  STAMP(17);
  asm volatile("fused_pf_end:" ::: "memory");
}

extern "C" void kernel_launch(void* const* d_in, const int* in_sizes, int n_in, void* d_out, int out_size, void* d_ws, size_t ws_size,
                              hipStream_t stream) {
  (void)in_sizes; (void)n_in; (void)out_size; (void)ws_size;
  (void)hipFuncSetAttribute((const void*)fused_block, hipFuncAttributeMaxDynamicSharedMemorySize, LDS_BYTES);
  fused_block<<<NWG, NTHR, LDS_BYTES, stream>>>((const u16*)d_in[0], (const u16*)d_in[1], (const u16*)d_in[2], (const u16*)d_in[4],
                                                (const u16*)d_in[5], (const u16*)d_in[6], (const u16*)d_in[7], (const u16*)d_in[8],
                                                (const u16*)d_in[9], (u16*)d_out, (unsigned char*)d_ws);
}
```

```cpp
#include <hip/hip_runtime.h>

typedef unsigned short u16;
typedef unsigned int u32;
typedef unsigned long long u64;
typedef u32 v4u __attribute__((ext_vector_type(4)));
typedef u32 v2u __attribute__((ext_vector_type(2)));
typedef float f32x4 __attribute__((ext_vector_type(4)));
typedef float f32x2 __attribute__((ext_vector_type(2)));
typedef short s16x8 __attribute__((ext_vector_type(8)));
typedef short s16x4 __attribute__((ext_vector_type(4)));
typedef __bf16 bf16x2 __attribute__((ext_vector_type(2)));
#define GAS __attribute__((address_space(1)))
#define LAS __attribute__((address_space(3)))
#define DI __device__ __forceinline__
typedef __amdgpu_buffer_rsrc_t rsrc_t;

#define ABL_NOFILL1 0
#define ABL_NOPREP 0
#define ABL_NOBAR1 0
#define ABL_NOFILL3 0
#define ABL_NOSTAGE3 0
#define ABL_NOREFILL4 0
#define ABL_NOACT4 0
#define ABL_P 0
#define ABL_NORD1 0
#define ABL_NOJOB 0
#define XN 2

constexpr int NWG = 256, NTHR = 512;
constexpr u32 OFF_E = 0, OFF_F0 = 0x1000, OFF_F1 = 0xA000, OFF_F2 = 0x12000, OFF_F3 = 0x1A000;
constexpr u32 OFF_MODP = 0x30000, OFF_WQ = 0x40000, OFF_WO = 0x300000, OFF_W1 = 0x400000, OFF_W2 = 0x700000;
constexpr u32 OFF_O = 0xA00000, OFF_XC = 0xD00000, OFF_HH = 0x1000000, OFF_MOM = 0x16C0000, OFF_UVZ = 0x17C0000, OFF_STAT = 0x17C8000, WS_USED = 0x1800000;
constexpr u32 OFF_FU = 0x22000, OFF_F0B = 0x26000;
constexpr u32 OFF_DIAG = 0x1800000;
constexpr int L_A = 0, L_OB = 39424, L_WO = 51200, L_JOB = 102400, L_GV = 143360, L_CNT = 145408, L_RED = 0, L_RED4 = 102400, L_SM = 153600, L_VEC = 154624, LDS_BYTES = 163840;
constexpr int L_SPK = 154624, L_CPK = 155904, L_ONE = 157184, L_CONST = 157216, L_CF = 157312, L_ROWC = 157696, L_UVZ = 158720, L_STAT = 161280;
constexpr int QS = 488;
constexpr int OS = 176;
constexpr int SPIN_CAP = 1 << 14;
constexpr int SC1 = 16;

DI f32x4 mfma(v4u a, v4u b, f32x4 c) {
  return __builtin_amdgcn_mfma_f32_16x16x32_bf16(__builtin_bit_cast(s16x8, a), __builtin_bit_cast(s16x8, b), c, 0, 0, 0);
}
#define USE_AGPR 0
DI void mfma_acc(f32x4& c, v4u a, v4u b) {
  c = mfma(a, b, c);
}
#define MFMA_DONE() asm volatile("s_nop 7\n\ts_nop 3" ::: "memory")
DI u32 pack2(float a, float b) { f32x2 v = {a, b}; return __builtin_bit_cast(u32, __builtin_convertvector(v, bf16x2)); }
DI float blo(u32 u) { return __builtin_bit_cast(float, u << 16); }
DI float bhi(u32 u) { return __builtin_bit_cast(float, u & 0xffff0000u); }
DI f32x2 unpk(u32 u) { return f32x2{blo(u), bhi(u)}; }
DI f32x2 fma2(f32x2 a, f32x2 b, f32x2 c) { return __builtin_elementwise_fma(a, b, c); }
DI u32 pk(f32x2 v) { return __builtin_bit_cast(u32, __builtin_convertvector(v, bf16x2)); }
DI float dot2(u32 a, u32 b, float c) { return __builtin_amdgcn_fdot2_f32_bf16(__builtin_bit_cast(bf16x2, a), __builtin_bit_cast(bf16x2, b), c, false); }
DI float fma1(float a, float b, float c) { float r; asm("v_fma_f32 %0, %1, %2, %3" : "=v"(r) : "v"(a), "v"(b), "v"(c)); return r; }
DI float bf2f(u16 v) { return __builtin_bit_cast(float, (u32)v << 16); }
template <int CTRL> DI float dpp_add(float v) {
  return v + __builtin_bit_cast(float, __builtin_amdgcn_update_dpp(0, __builtin_bit_cast(int, v), CTRL, 0xf, 0xf, true));
}
DI float row16_sum(float v) { v = dpp_add<0x128>(v); v = dpp_add<0x124>(v); v = dpp_add<0x122>(v); return dpp_add<0x121>(v); }
DI float x16_sum(float v) {
  auto r = __builtin_amdgcn_permlane16_swap(__builtin_bit_cast(u32, v), __builtin_bit_cast(u32, v), false, false);
  const u32 a = r[0], c = r[1];
  return __builtin_bit_cast(float, a) + __builtin_bit_cast(float, c);
}
DI float x32_sum(float v) {
  auto r = __builtin_amdgcn_permlane32_swap(__builtin_bit_cast(u32, v), __builtin_bit_cast(u32, v), false, false);
  const u32 a = r[0], c = r[1];
  return __builtin_bit_cast(float, a) + __builtin_bit_cast(float, c);
}
DI float x16_max(float v) {
  auto r = __builtin_amdgcn_permlane16_swap(__builtin_bit_cast(u32, v), __builtin_bit_cast(u32, v), false, false);
  const u32 a = r[0], c = r[1];
  return fmaxf(__builtin_bit_cast(float, a), __builtin_bit_cast(float, c));
}
DI float x32_max(float v) {
  auto r = __builtin_amdgcn_permlane32_swap(__builtin_bit_cast(u32, v), __builtin_bit_cast(u32, v), false, false);
  const u32 a = r[0], c = r[1];
  return fmaxf(__builtin_bit_cast(float, a), __builtin_bit_cast(float, c));
}
DI float swap16_add(float a, float b) {
  auto r = __builtin_amdgcn_permlane16_swap(__builtin_bit_cast(u32, a), __builtin_bit_cast(u32, b), false, false);
  const u32 x = r[0], y = r[1];
  return __builtin_bit_cast(float, x) + __builtin_bit_cast(float, y);
}
DI float swap32_add(float a, float b) {
  auto r = __builtin_amdgcn_permlane32_swap(__builtin_bit_cast(u32, a), __builtin_bit_cast(u32, b), false, false);
  const u32 x = r[0], y = r[1];
  return __builtin_bit_cast(float, x) + __builtin_bit_cast(float, y);
}
template <int V> struct IntC { static constexpr int value = V; };
template <bool V> struct BoolC { static constexpr bool value = V; };
template <int N, int I = 0, class F> DI void static_for(F&& f) {
  if constexpr (I < N) { f(IntC<I>{}); static_for<N, I + 1>(f); }
}
DI void bar() { asm volatile("s_waitcnt lgkmcnt(0)\n\ts_barrier" ::: "memory"); }
DI void drain() { asm volatile("s_waitcnt vmcnt(0)" ::: "memory"); }
#define SCHED() __builtin_amdgcn_sched_barrier(0)
#define SGB(mask, n) __builtin_amdgcn_sched_group_barrier(mask, n, 0)
#define VMCNT(n) asm volatile("s_waitcnt vmcnt(" #n ")" ::: "memory")
DI v4u ldw(rsrc_t rs, u32 voff, u32 soff) { return __builtin_amdgcn_raw_buffer_load_b128(rs, voff, soff, SC1); }
DI void stw(rsrc_t rs, v4u v, u32 voff, u32 soff) { __builtin_amdgcn_raw_buffer_store_b128(v, rs, voff + soff, 0, SC1); }
DI v4u lds128(LAS unsigned char* L, int off) { return *(LAS v4u*)(L + off); }
DI f32x4 ldsf4(LAS unsigned char* L, int off) { return *(LAS f32x4*)(L + off); }
DI v2u lds64(LAS unsigned char* L, int off) { return *(LAS v2u*)(L + off); }
DI v2u ldstr(LAS unsigned char* L, int off) {
  return __builtin_bit_cast(v2u, __builtin_amdgcn_ds_read_tr16_b64_v4i16((LAS s16x4*)(L + off)));
}
template <int N> DI void dma(rsrc_t rs, u32 voff, u32 soff, u32 ldsoff) {
#define DMA_I(o) "buffer_load_dwordx4 %1, %2, %3 offen offset:" #o " sc1 lds\n\t"
#define DMA_ARGS :: "s"(ldsoff), "v"(voff), "s"(rs), "s"(soff) : "memory"
  if constexpr (N == 4) asm volatile("s_mov_b32 m0, %0\n\ts_nop 0\n\t" DMA_I(0) DMA_I(1024) DMA_I(2048) DMA_I(3072) DMA_ARGS);
  else if constexpr (N == 3) asm volatile("s_mov_b32 m0, %0\n\ts_nop 0\n\t" DMA_I(0) DMA_I(1024) DMA_I(2048) DMA_ARGS);
  else if constexpr (N == 2) asm volatile("s_mov_b32 m0, %0\n\ts_nop 0\n\t" DMA_I(0) DMA_I(1024) DMA_ARGS);
  else asm volatile("s_mov_b32 m0, %0\n\ts_nop 0\n\t" DMA_I(0) DMA_ARGS);
}
DI void poll(unsigned char* ws, u32 off, int n, int lane, u64 T) {
  const GAS u64* f = (const GAS u64*)(ws + off + (lane < n ? lane : 0) * 128);
#pragma unroll 1
  for (int spins = 0; spins < SPIN_CAP; ++spins) {
    const u64 v = __hip_atomic_load(f, __ATOMIC_RELAXED, __HIP_MEMORY_SCOPE_AGENT);
    if (__all(v == T)) break;
    __builtin_amdgcn_s_sleep(1);
  }
  __builtin_amdgcn_fence(__ATOMIC_ACQUIRE, "wavefront");
}
DI void poll256(unsigned char* ws, u32 off, int lane, u64 T) {
  const GAS u64* f = (const GAS u64*)(ws + off + lane * 128);
#pragma unroll 1
  for (int spins = 0; spins < SPIN_CAP; ++spins) {
    const u64 v0 = __hip_atomic_load(f, __ATOMIC_RELAXED, __HIP_MEMORY_SCOPE_AGENT);
    const u64 v1 = __hip_atomic_load(f + 1024, __ATOMIC_RELAXED, __HIP_MEMORY_SCOPE_AGENT);
    const u64 v2 = __hip_atomic_load(f + 2048, __ATOMIC_RELAXED, __HIP_MEMORY_SCOPE_AGENT);
    const u64 v3 = __hip_atomic_load(f + 3072, __ATOMIC_RELAXED, __HIP_MEMORY_SCOPE_AGENT);
    if (__all(v0 == T && v1 == T && v2 == T && v3 == T)) break;
    __builtin_amdgcn_s_sleep(1);
  }
  __builtin_amdgcn_fence(__ATOMIC_ACQUIRE, "wavefront");
}
DI void publish(unsigned char* ws, u32 off, u64 T) {
  __hip_atomic_store((GAS u64*)(ws + off), T, __ATOMIC_RELAXED, __HIP_MEMORY_SCOPE_AGENT);
}
DI void row_stats(const v4u (&r)[5], float& rstd, float& nb) {
  float s0 = 0.f, s1 = 0.f, q0 = 0.f, q1 = 0.f;
#pragma unroll
  for (int m = 0; m < 5; ++m) {
    s0 = dot2(r[m].x, 0x3f803f80u, s0); q0 = dot2(r[m].x, r[m].x, q0);
    s1 = dot2(r[m].y, 0x3f803f80u, s1); q1 = dot2(r[m].y, r[m].y, q1);
    s0 = dot2(r[m].z, 0x3f803f80u, s0); q0 = dot2(r[m].z, r[m].z, q0);
    s1 = dot2(r[m].w, 0x3f803f80u, s1); q1 = dot2(r[m].w, r[m].w, q1);
  }
  const float mean = row16_sum(s0 + s1) * (1.f / 640.f);
  const float var = fmaxf(row16_sum(q0 + q1) * (1.f / 640.f) - mean * mean, 0.f);
  rstd = __builtin_amdgcn_rsqf(var + 1e-5f);
  nb = -mean * rstd;
}

#define STAMP(i) asm volatile("; MARK " #i ::: "memory")
#define STAMPW(i, ww) do {} while (0)

typedef const GAS unsigned char* gptr;
__global__ __launch_bounds__(NTHR, 2) void fused_block(const u16* a_x, const u16* a_wqkv, const u16* a_wo, const u16* a_w1, const u16* a_b1,
                                                       const u16* a_w2, const u16* a_b2, const u16* a_c, const u16* a_wmod, u16* a_out,
                                                       unsigned char* a_ws) {
  extern __shared__ __attribute__((aligned(16))) unsigned char smem[];
  const __attribute__((address_space(4))) u64* ka = (const __attribute__((address_space(4))) u64*)__builtin_amdgcn_kernarg_segment_ptr();
  u64 k0 = ka[0], k1 = ka[1], k2 = ka[2], k3 = ka[3], k4 = ka[4], k5 = ka[5], k6 = ka[6], k7 = ka[7], k8 = ka[8], k9 = ka[9], k10 = ka[10];
  asm volatile("" : "+s"(k0), "+s"(k1), "+s"(k2), "+s"(k3), "+s"(k4), "+s"(k5), "+s"(k6), "+s"(k7), "+s"(k8), "+s"(k9), "+s"(k10));
  const gptr x = (gptr)k0, wqkv = (gptr)k1, wo = (gptr)k2, w1 = (gptr)k3, b1 = (gptr)k4, w2 = (gptr)k5, b2 = (gptr)k6, cvec = (gptr)k7, wmod = (gptr)k8;
  GAS unsigned char* const out = (GAS unsigned char*)k9;
  unsigned char* const ws = (unsigned char*)k10;
  LAS unsigned char* L = (LAS unsigned char*)smem;
  const int tid = threadIdx.x, lane = tid & 63, b = blockIdx.x;
  const int w = __builtin_amdgcn_readfirstlane(tid >> 6);
  const int xcd = b & 7, jj = b >> 3;
  const int p = (xcd >> 1) * 8 + (jj >> 2), g = (xcd & 1) * 4 + (jj & 3);
  const rsrc_t rs = __builtin_amdgcn_make_buffer_rsrc(ws, 0, WS_USED, 0x00020000);
  const int g4 = lane >> 4, t16 = lane & 15;
  const u32 l16 = lane * 16;
  STAMP(0);

  const u64 Ev = __hip_atomic_load((const GAS u64*)(ws + OFF_E), __ATOMIC_RELAXED, __HIP_MEMORY_SCOPE_AGENT);
  if (tid == 0) { *(LAS u32*)(L + L_CNT) = 0u; *(LAS u32*)(L + L_CNT + 4) = 0u; }
  bar();
  const int id2 = (w - 4) * 256 + b;
  const bool hasjob = w < 4 ? w * 256 + b < 640 : id2 < 972;
  const int job = w < 4 ? (hasjob ? w * 256 + b : 0) : (id2 < 540 ? 1072 + id2 : (hasjob ? 640 + id2 - 540 : 1072));
  gptr jsrc; u32 jstride, jdst, jA, jB; int ppr; bool paired;
  if (job < 480) {
    const int gg = job / 60, rem = job % 60, part = rem / 20, j = rem % 20;
    jsrc = wqkv + (u32)(32 * j * 3840 + part * 1280 + gg * 160);
    jstride = 3840; ppr = 10; paired = false;
    jdst = OFF_WQ + (gg * 15 + part * 5) * 20480 + j * 1024; jA = 40960; jB = 20480;
  } else if (job < 640) {
    const int id = job - 480, gg = id / 20, j = id % 20;
    jsrc = wo + (u32)(32 * j * 1280 + gg * 160);
    jstride = 1280; ppr = 10; paired = true;
    jdst = OFF_WO + (gg * 20 + j) * 5120; jA = 2048; jB = 1024;
  } else if (job < 1072) {
    const int id = job - 640, gg = id / 54, j = id % 54;
    jsrc = w2 + (u32)(32 * j * 1280 + gg * 160);
    jstride = 1280; ppr = 10; paired = true;
    jdst = OFF_W2 + (gg * 54 + j) * 5120; jA = 2048; jB = 1024;
  } else {
    const int id = job - 1072, jb2 = id / 20, j = id % 20;
    jsrc = w1 + (u32)(32 * j * 3456 + jb2 * 128);
    jstride = 3456; ppr = 8; paired = true;
    jdst = OFF_W1 + (2 * jb2 * 20 + j) * 2048; jA = 40960; jB = 1024;
  }
    const int cl = b % 60, ks = b < 240 ? b / 60 : 3;
  v4u jr[5];
  auto job_loads = [&]() {
    const u32 magic = ppr == 10 ? 6554u : 8192u;
#pragma unroll
    for (int it = 0; it < 5; ++it) {
      u32 idx = it * 64 + lane;
      if (it == 4 && ppr == 8) idx = lane;
      const u32 row = (idx * magic) >> 16, pc = idx - row * ppr;
      jr[it] = *(const GAS v4u*)(jsrc + (row * jstride + pc * 16));
      SCHED();
    }
  };
  auto run_job = [&]() {
    LAS unsigned char* sc = L + L_JOB + w * 5120;
#pragma unroll
    for (int it = 0; it < 5; ++it)
      if (it < 4 || ppr == 10) *(LAS v4u*)(sc + it * 1024 + l16) = jr[it];
    __builtin_amdgcn_fence(__ATOMIC_RELEASE, "wavefront");
    __builtin_amdgcn_wave_barrier();
    __builtin_amdgcn_fence(__ATOMIC_ACQUIRE, "wavefront");
    const int rowb = ppr * 16;
    const int q = t16 >> 2, pp = lane & 3;
    const int rbase = (8 * g4 + q) * rowb;
#pragma unroll
    for (int ct = 0; ct < 5; ++ct) {
      if (ct < 4 || ppr == 10) {
        const int cb = (paired && ct < 4) ? 64 * (ct >> 1) + 8 * (ct & 1) + 16 * pp : 32 * ct + 8 * pp;
        const v2u lo = ldstr(sc, rbase + cb), hi = ldstr(sc, rbase + 4 * rowb + cb);
        stw(rs, v4u{lo.x, lo.y, hi.x, hi.y}, l16, jdst + (ct >> 1) * jA + (ct & 1) * jB);
      }
    }
  };
  v4u xr[2][5]; v2u xh[5];
  u32 xo[3];
#pragma unroll
  for (int a = 0; a < 3; ++a) {
    const int tok = 64 * p - 16 + 4 * (a < 2 ? w + 8 * a : (w & 3) + 16) + g4;
    xo[a] = (tok < 0 ? 0 : tok) * 1280 + t16 * 16 + (a == 2 ? (w >> 2) * 8 : 0);
  }
  auto x_chunk = [&](int m) {
    xr[0][m] = *(const GAS v4u*)(x + (xo[0] + m * 256)); SCHED();
    xr[1][m] = *(const GAS v4u*)(x + (xo[1] + m * 256)); SCHED();
    xh[m] = *(const GAS v2u*)(x + (xo[2] + m * 256)); SCHED();
  };
  auto lds_add = [&](int off) { __hip_atomic_fetch_add((LAS u32*)(L + off), 1u, __ATOMIC_RELAXED, __HIP_MEMORY_SCOPE_WORKGROUP); };
  auto lds_wait = [&](int off, u32 n) {
#pragma unroll 1
    for (int spins = 0; spins < SPIN_CAP * 16; ++spins) {
      if (__hip_atomic_load((LAS u32*)(L + off), __ATOMIC_RELAXED, __HIP_MEMORY_SCOPE_WORKGROUP) >= n) break;
      __builtin_amdgcn_s_sleep(1);
    }
    asm volatile("" ::: "memory");
  };
  if (w < 4) {
    v4u gw[5];
#pragma unroll
    for (int i = 0; i < 5; ++i) { gw[i] = *(const GAS v4u*)(wmod + (u32)((160 * ks + 40 * w + (lane >> 3) + 8 * i) * 7680 + cl * 128 + (lane & 7) * 16)); SCHED(); }
    const u16 gc = *(const GAS u16*)(cvec + (160 * ks + 40 * w + (lane < 40 ? lane : 0)) * 2);
    SCHED();
    v4u sr[10];
    if (w == 3) {
#pragma unroll
      for (int i = 0; i < 10; ++i) { sr[i] = *(const GAS v4u*)(x + (u32)((8 * b + (lane >> 3)) * 1280 + ((lane & 7) + 8 * i) * 16)); SCHED(); }
    } else {
      job_loads();
    }
    STAMP(18);
    const float c = bf2f(gc);
    const float sl = c * __builtin_amdgcn_rcpf(1.f + __expf(-c));
    f32x2 acc[4];
#pragma unroll
    for (int i = 0; i < 5; ++i) {
      const float sc = __builtin_bit_cast(float, __builtin_amdgcn_ds_bpermute(((lane >> 3) + 8 * i) * 4, __builtin_bit_cast(int, sl)));
      const f32x2 s2 = {sc, sc};
#pragma unroll
      for (int e = 0; e < 4; ++e) acc[e] = i == 0 ? unpk(gw[i][e]) * s2 : fma2(unpk(gw[i][e]), s2, acc[e]);
    }
    float u[8];
#pragma unroll
    for (int e = 0; e < 4; ++e) { u[2 * e] = dpp_add<0x128>(acc[e].x); u[2 * e + 1] = dpp_add<0x128>(acc[e].y); }
    float v[4];
#pragma unroll
    for (int e = 0; e < 4; ++e) v[e] = swap16_add(u[2 * e], u[2 * e + 1]);
    const float z0 = swap32_add(v[0], v[1]), z1 = swap32_add(v[2], v[3]);
    const float z = (lane & 8) ? z1 : z0;
    const int col = (lane & 7) * 8 + ((lane >> 1) & 4) + ((lane >> 4) & 2) + ((lane >> 4) & 1);
    *(LAS float*)(L + L_GV + w * 256 + col * 4) = z;
    asm volatile("s_waitcnt lgkmcnt(0)" ::: "memory");
    if (lane == 0) lds_add(L_CNT);
    STAMP(19);
    if (w == 3) {
      float s0 = 0.f, s1 = 0.f, q0 = 0.f, q1 = 0.f;
#pragma unroll
      for (int i = 0; i < 10; ++i) {
        s0 = dot2(sr[i].x, 0x3f803f80u, s0); q0 = dot2(sr[i].x, sr[i].x, q0); s1 = dot2(sr[i].y, 0x3f803f80u, s1); q1 = dot2(sr[i].y, sr[i].y, q1);
        s0 = dot2(sr[i].z, 0x3f803f80u, s0); q0 = dot2(sr[i].z, sr[i].z, q0); s1 = dot2(sr[i].w, 0x3f803f80u, s1); q1 = dot2(sr[i].w, sr[i].w, q1);
      }
      const float mean = dpp_add<0x141>(dpp_add<0x4e>(dpp_add<0xb1>(s0 + s1))) * (1.f / 640.f);
      const float var = fmaxf(dpp_add<0x141>(dpp_add<0x4e>(dpp_add<0xb1>(q0 + q1))) * (1.f / 640.f) - mean * mean, 0.f);
      const float rstd = __builtin_amdgcn_rsqf(var + 1e-5f);
      if ((lane & 7) == 0)
        __builtin_amdgcn_raw_buffer_store_b64(__builtin_bit_cast(v2u, f32x2{rstd, -mean * rstd}), rs, (8 * b + (lane >> 3)) * 8, OFF_STAT, SC1);
      lds_wait(L_CNT, 4);
      const float s = (*(LAS float*)(L + L_GV + lane * 4) + *(LAS float*)(L + L_GV + 256 + lane * 4)) +
                      (*(LAS float*)(L + L_GV + 512 + lane * 4) + *(LAS float*)(L + L_GV + 768 + lane * 4));
      if (b < 240) __builtin_amdgcn_raw_buffer_store_b32(__builtin_bit_cast(u32, s), rs, lane * 4, OFF_MODP + (ks * 3840 + cl * 64) * 4, SC1);
    } else if (hasjob) {
      run_job();
    }
    drain();
    if (lane == 0) lds_add(L_CNT + 4);
    if (w == 3) {
      lds_wait(L_CNT + 4, 4);
      u64 e3 = Ev;
      asm volatile("" : "+v"(e3));
      if (lane == 0) publish(ws, OFF_F0 + b * 128, e3 + 1);
      STAMPW(20, 3);
    } else {
      x_chunk(0); x_chunk(1); x_chunk(2);
    }
  } else {
    if (w == 7) {
      u64 pc; int len;
      asm volatile("fused_pf_begin:\n\ts_getpc_b64 %0\n\ts_mov_b32 %1, fused_pf_end-fused_pf_begin" : "=s"(pc), "=s"(len));
      const int o = (jj * 16 + t16) * 128;
      u32 pf = 0;
      if (o < len) pf = *(const GAS u32*)(pc + (u32)o);
      asm volatile("" ::"v"(pf));
    }
    lds_wait(L_CNT, 4);
    x_chunk(0); x_chunk(1); x_chunk(2);
  }
  if (w == 3) {
    u64 e3 = Ev;
    asm volatile("" : "+v"(e3));
    poll256(ws, OFF_F0, lane, e3 + 1);
    STAMPW(22, 3);
    if (b == 0 && lane == 0) publish(ws, OFF_E, e3 + 1);
    v4u pv1[5][4]; v2u st[2];
#pragma unroll
    for (int r = 0; r < 5; ++r)
#pragma unroll
      for (int k = 0; k < 4; ++k) { pv1[r][k] = ldw(rs, (r * 64 + lane) * 16, OFF_MODP + k * 15360); SCHED(); }
#pragma unroll
    for (int r = 0; r < 2; ++r) {
      int tok = 64 * p - 16 + r * 64 + (r == 0 || lane < 16 ? lane : 0);
      tok = tok < 0 ? 0 : tok;
      st[r] = __builtin_amdgcn_raw_buffer_load_b64(rs, tok * 8, OFF_STAT, SC1);
    }
    SCHED();
    x_chunk(0); x_chunk(1); x_chunk(2);
#pragma unroll
    for (int r = 0; r < 5; ++r) {
      const f32x4 s = (__builtin_bit_cast(f32x4, pv1[r][0]) + __builtin_bit_cast(f32x4, pv1[r][1])) + (__builtin_bit_cast(f32x4, pv1[r][2]) + __builtin_bit_cast(f32x4, pv1[r][3]));
      const int qd = r * 64 + lane, col = (qd < 160 ? qd : qd - 160) * 4;
      const int vo = L_VEC + (col >> 3) * 80 + (col & 4) * 4;
      if (qd < 160) *(LAS f32x4*)(L + vo + 32) = s;
      else *(LAS f32x4*)(L + vo) = s + 1.f;
    }
    *(LAS v2u*)(L + L_STAT + lane * 8) = st[0];
    if (lane < 16) *(LAS v2u*)(L + L_STAT + 512 + lane * 8) = st[1];
  }
  STAMP(2);
  STAMPW(23, 3);
  u64 Tv = Ev;
  asm volatile("" : "+v"(Tv));
  Tv += 1;
  const u32 Tlo = __builtin_amdgcn_readfirstlane((u32)Tv), Thi = __builtin_amdgcn_readfirstlane((u32)(Tv >> 32));
  const u64 T = ((u64)Thi << 32) | Tlo;
  bar();
  STAMPW(25, 0);

  constexpr int R1 = 6;
  v4u wr[R1][2];
  const u32 wq0 = OFF_WQ + (g * 15 + 2 * w) * 20480, wq1 = OFF_WQ + (g * 15 + (w < 7 ? 2 * w + 1 : 14)) * 20480;
#pragma unroll
  for (int j = 0; j < R1; ++j) {
    if (j == 2 || j == 4) asm volatile("s_barrier" ::: "memory");
    wr[j][0] = ldw(rs, l16 + (j & 3) * 1024, wq0 + (j >> 2) * 4096); wr[j][1] = ldw(rs, l16 + (j & 3) * 1024, wq1 + (j >> 2) * 4096);
    SCHED();
  }
  STAMPW(26, 0);
  f32x4 acc1[5][2];
#pragma unroll
  for (int i = 0; i < 5; ++i) { acc1[i][0] = f32x4{0.f, 0.f, 0.f, 0.f}; acc1[i][1] = f32x4{0.f, 0.f, 0.f, 0.f}; }
  const int rd_even = g4 * 256 + ((t16 ^ g4) * 16), rd_odd = rd_even ^ 64;
  int wad[3];
  float xrs[3], xnb[3];
#pragma unroll
  for (int a = 0; a < 3; ++a) {
    const int grp = a < 2 ? w + 8 * a : (w & 3) + 16, tt = grp >> 2, t = 4 * (grp & 3) + g4;
    wad[a] = L_A + tt * 20480 + (t16 >> 2) * 1024 + (t16 & 3) * 256 + ((t ^ (t16 & 7)) * 16) + (a == 2 ? (w >> 2) * 8 : 0);
    const v2u st = lds64(L, L_STAT + (4 * grp + g4) * 8);
    xrs[a] = __builtin_bit_cast(float, (u32)st.x); xnb[a] = __builtin_bit_cast(float, (u32)st.y);
  }
  const int vad = L_VEC + t16 * 80, vah = vad + (w >> 2) * 16;
  f32x4 cm[3], sv[3];
  auto ldmod = [&](int m) {
    cm[0] = ldsf4(L, vad + m * 1280); sv[0] = ldsf4(L, vad + m * 1280 + 32);
    cm[1] = ldsf4(L, vad + m * 1280 + 16); sv[1] = ldsf4(L, vad + m * 1280 + 48);
    cm[2] = ldsf4(L, vah + m * 1280); sv[2] = ldsf4(L, vah + m * 1280 + 32);
  };
#pragma unroll
  for (int m = 0; m < 2; ++m) {
    ldmod(m);
#pragma unroll
    for (int a = 0; a < 2; ++a) {
      const f32x2 r2 = {xrs[a], xrs[a]}, n2 = {xnb[a], xnb[a]};
      *(LAS v4u*)(L + wad[a] + m * 4096) =
          v4u{pk(fma2(fma2(unpk(xr[a][m].x), r2, n2), cm[0].xy, sv[0].xy)), pk(fma2(fma2(unpk(xr[a][m].y), r2, n2), cm[0].zw, sv[0].zw)),
              pk(fma2(fma2(unpk(xr[a][m].z), r2, n2), cm[1].xy, sv[1].xy)), pk(fma2(fma2(unpk(xr[a][m].w), r2, n2), cm[1].zw, sv[1].zw))};
    }
    const f32x2 r2 = {xrs[2], xrs[2]}, n2 = {xnb[2], xnb[2]};
    *(LAS v2u*)(L + wad[2] + m * 4096) = v2u{pk(fma2(fma2(unpk(xh[m].x), r2, n2), cm[2].xy, sv[2].xy)), pk(fma2(fma2(unpk(xh[m].y), r2, n2), cm[2].zw, sv[2].zw))};
  }
  bar();
  STAMP(4);
  v4u pvs[4];
  {
    int rdA[2] = {L_A + rd_even, L_A + rd_odd}, rdB[2] = {L_A + 81920 + rd_even, L_A + 81920 + rd_odd};
    asm volatile("" : "+v"(rdA[0]), "+v"(rdA[1]), "+v"(rdB[0]), "+v"(rdB[1]));
    auto rd = [&](int tt, int j) { return tt < 4 ? lds128(L, rdA[j & 1] + tt * 20480 + j * 1024) : lds128(L, rdB[j & 1] + j * 1024); };
    v4u af[2][5];
#pragma unroll
    for (int tt = 0; tt < 5; ++tt) af[0][tt] = rd(tt, 0);
    SCHED();
    float pu[8]; v4u po;
    auto uop = [&](auto oc, auto mc) {
      constexpr int o = decltype(oc)::value, mp = decltype(mc)::value;
      if constexpr (o < 58) {
        constexpr int a = o / 29, i = o % 29;
        constexpr int kind[29] = {0, 0, 0, 0, 1, 1, 0, 0, 1, 1, 0, 0, 2, 2, 1, 1, 2, 2, 1, 1, 3, 2, 2, 3, 2, 2, 3, 3, 4};
        constexpr int el[29] = {0, 1, 2, 3, 0, 1, 4, 5, 2, 3, 6, 7, 0, 1, 4, 5, 2, 3, 6, 7, 0, 4, 5, 1, 6, 7, 2, 3, 0};
        constexpr int e = el[i];
        if constexpr (kind[i] == 0) pu[e] = (e & 1) ? bhi(xr[a][mp][e >> 1]) : blo(xr[a][mp][e >> 1]);
        else if constexpr (kind[i] == 1) pu[e] = fma1(pu[e], xrs[a], xnb[a]);
        else if constexpr (kind[i] == 2) pu[e] = fma1(pu[e], cm[e >> 2][e & 3], sv[e >> 2][e & 3]);
        else if constexpr (kind[i] == 3) po[e] = pk(f32x2{pu[2 * e], pu[2 * e + 1]});
        else *(LAS v4u*)(L + wad[a] + mp * 4096) = po;
      } else {
        constexpr int i = o - 58;
        constexpr int kind[15] = {0, 0, 0, 0, 1, 1, 1, 1, 2, 2, 2, 2, 3, 3, 4};
        constexpr int el[15] = {0, 1, 2, 3, 0, 1, 2, 3, 0, 1, 2, 3, 0, 1, 0};
        constexpr int e = el[i];
        if constexpr (kind[i] == 0) pu[e] = (e & 1) ? bhi(xh[mp][e >> 1]) : blo(xh[mp][e >> 1]);
        else if constexpr (kind[i] == 1) pu[e] = fma1(pu[e], xrs[2], xnb[2]);
        else if constexpr (kind[i] == 2) pu[e] = fma1(pu[e], cm[2][e], sv[2][e]);
        else if constexpr (kind[i] == 3) po[e] = pk(f32x2{pu[2 * e], pu[2 * e + 1]});
        else *(LAS v2u*)(L + wad[2] + mp * 4096) = v2u{po.x, po.y};
      }
    };
    static_for<20>([&](auto jc) {
      constexpr int j = decltype(jc)::value;
      constexpr int m = j >> 2, q = j & 3, cu = j & 1, nx = cu ^ 1, mp = m + 2 < 5 ? m + 2 : 4;
      constexpr bool more = j + 1 < 20 && !ABL_NORD1, fill = j + R1 < 20 && !ABL_NOFILL1, prep = m < 3 && !ABL_NOPREP;
      if constexpr (j == 1 && !ABL_NOJOB) { if (w >= 4) job_loads(); }
      if constexpr (j == 9 && !ABL_NOJOB) { if (w >= 4 && hasjob) run_job(); }
      if constexpr (j == 16) {
        const u32 vo = 1920 * 4 + (tid < 320 ? tid : 0) * 16;
#pragma unroll
        for (int k = 0; k < 4; ++k) { pvs[k] = ldw(rs, vo, OFF_MODP + k * 15360); SCHED(); }
      }
      const v4u wa = wr[j % R1][0], wb = wr[j % R1][1];
      static_for<10>([&](auto sc) {
        constexpr int sl = decltype(sc)::value, tt = sl >> 1, S = q * 10 + sl;
        mfma_acc(acc1[tt][sl & 1], (sl & 1) ? wb : wa, af[cu][tt]);
        if constexpr (more && !(sl & 1)) af[nx][tt] = rd(tt, j + 1);
        if constexpr (fill && sl == 3) wr[j % R1][0] = ldw(rs, l16 + ((j + R1) & 3) * 1024, wq0 + ((j + R1) >> 2) * 4096);
        if constexpr (fill && sl == 7) wr[j % R1][1] = ldw(rs, l16 + ((j + R1) & 3) * 1024, wq1 + ((j + R1) >> 2) * 4096);
        if constexpr (prep && S == 0) ldmod(mp);
        if constexpr (m < 2 && S == 5) x_chunk(m + 3);
        if constexpr (prep && S >= 2) {
          constexpr int o0 = (S - 2) * 73 / 38, o1 = (S - 1) * 73 / 38;
          static_for<o1 - o0>([&](auto oc) { uop(IntC<o0 + decltype(oc)::value>{}, IntC<mp>{}); });
        }
        SCHED();
      });
      if constexpr (m < 3 && q == 3 && !ABL_NOBAR1) bar();
    });
  }
  MFMA_DONE();
  if (w >= 4) drain();
  bar();
  STAMP(5);
  if (tid == 256) publish(ws, OFF_F0B + b * 128, T);
  if (tid < 320) {
    f32x4 s = (__builtin_bit_cast(f32x4, pvs[0]) + __builtin_bit_cast(f32x4, pvs[1])) + (__builtin_bit_cast(f32x4, pvs[2]) + __builtin_bit_cast(f32x4, pvs[3]));
    if (tid >= 160) s += 1.f;
    *(LAS v2u*)(L + L_SPK + tid * 8) = v2u{pk(s.xy), pk(s.zw)};
  } else if (tid < 324) {
    *(LAS u32*)(L + L_ONE + (tid - 320) * 4) = 0x3f803f80u;
    *(LAS u32*)(L + L_ONE + 16 + (tid - 320) * 4) = 0u;
  }
  auto wo_dma = [&](int i) { const int gi = w + 8 * i; if (gi < 25) dma<4>(rs, l16, OFF_WO + g * 102400 + gi * 4096, L_WO + gi * 4096); };
#pragma unroll
  for (int tt = 0; tt < 5; ++tt)
#pragma unroll
    for (int c = 0; c < 2; ++c)
      if (c == 0 || w < 7)
        *(LAS v2u*)(L + L_A + (tt * 16 + t16) * QS + (2 * w + c) * 32 + g4 * 8) =
            v2u{pack2(acc1[tt][c].x, acc1[tt][c].y), pack2(acc1[tt][c].z, acc1[tt][c].w)};
  wo_dma(0);
  bar();
  STAMP(6);
  {
    const int h = w & 3, par = w >> 2;
    const float cs = 0.22360679774997896f * 1.4426950408889634f;
    const int vq = t16 >> 2, vp = lane & 3;
    v4u qf[2], kf[2][2], vf0[2], vf1[2];
#pragma unroll
    for (int qt = 0; qt < 2; ++qt) {
      const int qo = L_A + (2 * (8 + 16 * qt + t16) + par) * QS + 40 * h + (g4 < 3 ? 16 * g4 : 0);
      const v2u qlo = lds64(L, qo), qhi = lds64(L, qo + 8);
      qf[qt] = v4u{g4 == 3 ? 0u : qlo.x, g4 == 3 ? 0u : qlo.y, g4 >= 2 ? 0u : qhi.x, g4 >= 2 ? 0u : qhi.y};
#pragma unroll
      for (int kt = 0; kt < 2; ++kt) {
        int ki = 16 * qt + 16 * kt + t16;
        ki = ki > 39 ? 39 : ki;
        const int ko = L_A + (2 * ki + par) * QS + 160 + 40 * h + 16 * g4;
        const v2u klo = lds64(L, ko), khi = lds64(L, ko + 8);
        kf[qt][kt] = v4u{klo.x, klo.y, khi.x, khi.y};
      }
      int ka2 = 16 * qt + 4 * g4 + vq, kb = ka2 + 16;
      kb = kb > 39 ? 39 : kb;
      const int va = L_A + (2 * ka2 + par) * QS + 320 + 40 * h, vb = L_A + (2 * kb + par) * QS + 320 + 40 * h;
      const v2u a0 = ldstr(L, va + 8 * vp), b0 = ldstr(L, vb + 8 * vp), a1 = ldstr(L, va + 32), b1x = ldstr(L, vb + 32);
      vf0[qt] = v4u{a0.x, a0.y, b0.x, b0.y}; vf1[qt] = v4u{a1.x, a1.y, b1x.x, b1x.y};
    }
    f32x4 sc[2][2];
#pragma unroll
    for (int qt = 0; qt < 2; ++qt)
#pragma unroll
      for (int kt = 0; kt < 2; ++kt) sc[qt][kt] = mfma(kf[qt][kt], qf[qt], f32x4{0.f, 0.f, 0.f, 0.f});
    wo_dma(1);
    const float NEG = -3.0e38f;
#pragma unroll
    for (int kt = 0; kt < 2; ++kt)
#pragma unroll
      for (int r = 0; r < 4; ++r) {
        const bool ok = (unsigned)(16 * kt + 4 * g4 + r - t16 - 1) < 8u;
        sc[0][kt][r] = ok ? sc[0][kt][r] : NEG;
        sc[1][kt][r] = ok ? sc[1][kt][r] : NEG;
      }
    if (p == 0) {
#pragma unroll
      for (int r = 0; r < 4; ++r) sc[0][0][r] = g4 >= 2 ? sc[0][0][r] : NEG;
    }
    float mx[2], sum[2];
#pragma unroll
    for (int qt = 0; qt < 2; ++qt)
      mx[qt] = fmaxf(fmaxf(fmaxf(sc[qt][0].x, sc[qt][0].y), fmaxf(sc[qt][0].z, sc[qt][0].w)), fmaxf(fmaxf(sc[qt][1].x, sc[qt][1].y), fmaxf(sc[qt][1].z, sc[qt][1].w)));
#pragma unroll
    for (int qt = 0; qt < 2; ++qt) mx[qt] = x16_max(mx[qt]);
#pragma unroll
    for (int qt = 0; qt < 2; ++qt) mx[qt] = x32_max(mx[qt]) * cs;
#pragma unroll
    for (int qt = 0; qt < 2; ++qt) {
#pragma unroll
      for (int kt = 0; kt < 2; ++kt)
#pragma unroll
        for (int r = 0; r < 4; ++r) sc[qt][kt][r] = __builtin_amdgcn_exp2f(sc[qt][kt][r] * cs - mx[qt]);
      const f32x4 t4 = sc[qt][0] + sc[qt][1];
      sum[qt] = (t4.x + t4.y) + (t4.z + t4.w);
    }
    wo_dma(2);
#pragma unroll
    for (int qt = 0; qt < 2; ++qt) sum[qt] = x16_sum(sum[qt]);
#pragma unroll
    for (int qt = 0; qt < 2; ++qt) sum[qt] = __builtin_amdgcn_rcpf(x32_sum(sum[qt]));
#pragma unroll
    for (int qt = 0; qt < 2; ++qt) {
      const f32x2 iv = {sum[qt], sum[qt]};
      const v4u pf4 = {pk(sc[qt][0].xy * iv), pk(sc[qt][0].zw * iv), pk(sc[qt][1].xy * iv), pk(sc[qt][1].zw * iv)};
      const f32x4 o0 = mfma(vf0[qt], pf4, f32x4{0.f, 0.f, 0.f, 0.f});
      const f32x4 o1 = mfma(vf1[qt], pf4, f32x4{0.f, 0.f, 0.f, 0.f});
      const int orow = L_OB + (par * 32 + 16 * qt + t16) * OS + 40 * h;
      *(LAS v2u*)(L + orow + 8 * g4) = v2u{pk(o0.xy), pk(o0.zw)};
      if (g4 == 0) *(LAS v2u*)(L + orow + 32) = v2u{pk(o1.xy), pk(o1.zw)};
    }
  }
  wo_dma(3);
  bar();
  STAMP(21);
  {
    const int tok = 2 * (lane & 31) + (lane >> 5);
#pragma unroll
    for (int i = 0; i < 2; ++i) {
      const int u = w + 8 * i;
      if (u < 10) {
        const int ch = 10 * g + u;
        const v4u v = lds128(L, L_OB + lane * OS + 16 * u);
        stw(rs, v, (tok >> 4) * 20480 + (tok & 15) * 16, OFF_O + p * 81920 + (ch >> 2) * 1024 + (ch & 3) * 256);
      }
    }
  }
  drain();
  bar();
  if (tid == 0) publish(ws, OFF_F1 + (p * 8 + g) * 128, T);
  STAMP(7);

  const int s4 = w & 3, kh = w >> 2;
  const int tokg = 64 * p + 16 * s4 + t16;
  const int fo = kh == 0 ? 8 * g4 : 32 + 8 * g4;
  const v4u xa = *(const GAS v4u*)(x + (u32)(tokg * 1280 + (80 * g + fo) * 2));
  const v2u xb = *(const GAS v2u*)(x + (u32)(tokg * 1280 + (80 * g + 64 + 4 * g4) * 2));
  v4u pv[4]; v2u b2r = {0, 0};
  if (tid < 40) {
    const u32 vo = (tid >= 20 ? 3200 * 4 + (tid - 20) * 16 : 1280 * 4 + tid * 16) + g * 320;
#pragma unroll
    for (int k = 0; k < 4; ++k) pv[k] = ldw(rs, vo, OFF_MODP + k * 15360);
  }
  if (tid >= 64 && tid < 84) b2r = *(const GAS v2u*)(b2 + (80 * g + 4 * (tid - 64)) * 2);
  if (tid >= 128 && tid < 148) {
    const v2u cp = lds64(L, L_CPK + (80 * g + 4 * (tid - 128)) * 2);
    *(LAS f32x4*)(L + L_CF + (tid - 128) * 16) = f32x4{blo(cp.x), bhi(cp.x), blo(cp.y), bhi(cp.y)};
  }
  if (w == 6) {
    if (b < 108) {
      poll256(ws, OFF_F0B, lane, T);
      v4u uw[20];
#pragma unroll
      for (int j = 0; j < 20; ++j) { uw[j] = ldw(rs, l16 + (j & 1) * 2048, OFF_W1 + (b >> 1) * 40960 + (b & 1) * 1024 + (j >> 1) * 4096); SCHED(); }
      int ua = t16 < 2 ? (t16 == 0 ? L_CPK : L_SPK) + g4 * 16 : L_ONE + (t16 == 2 ? 0 : 16);
      const int ui = t16 < 2 ? 64 : 0;
      f32x4 ud = {0.f, 0.f, 0.f, 0.f};
#pragma unroll
      for (int j = 0; j < 20; ++j) { ud = mfma(uw[j], lds128(L, ua), ud); ua += ui; }
      if (t16 < 3) stw(rs, __builtin_bit_cast(v4u, ud), (((b >> 1) * 3 + t16) * 32 + 8 * g4 + 4 * (b & 1)) * 4, OFF_UVZ);
    }
  } else if (w == 7) {
    poll256(ws, OFF_F0B, lane, T);
    poll(ws, OFF_F1 + p * 8 * 128, 8, lane, T);
  }
  if (tid < 40) {
    f32x4 s = {0.f, 0.f, 0.f, 0.f};
#pragma unroll
    for (int k = 0; k < 4; ++k) s += __builtin_bit_cast(f32x4, pv[k]);
    *(LAS f32x4*)(L + L_SM + tid * 16) = s;
  }
  if (tid >= 64 && tid < 84) *(LAS f32x4*)(L + L_SM + 640 + (tid - 64) * 16) = f32x4{blo(b2r.x), bhi(b2r.x), blo(b2r.y), bhi(b2r.y)};
  bar();
  STAMP(8);
  v4u oa[10];
#pragma unroll
  for (int i = 0; i < 4; ++i) { oa[i] = ldw(rs, l16 + (i & 3) * 1024, OFF_O + (p * 4 + s4) * 20480 + kh * 10240 + (i >> 2) * 4096); SCHED(); }
  SCHED();
  f32x4 acc2[5];
#pragma unroll
  for (int i = 0; i < 5; ++i) acc2[i] = f32x4{0.f, 0.f, 0.f, 0.f};
  {
    v4u wf[2][5];
    const int wb2 = L_WO + kh * 51200 + l16;
#pragma unroll
    for (int ct = 0; ct < 5; ++ct) wf[0][ct] = lds128(L, wb2 + ct * 1024);
    SCHED();
#pragma unroll
    for (int i = 0; i < 10; ++i) {
#pragma unroll
      for (int ct = 0; ct < 5; ++ct) {
        mfma_acc(acc2[ct], wf[i & 1][ct], oa[i]);
        if (i + 1 < 10) wf[(i + 1) & 1][ct] = lds128(L, wb2 + ((i + 1) * 5 + ct) * 1024);
        if (ct == 1 && i + 4 < 10) oa[i + 4] = ldw(rs, l16 + ((i + 4) & 3) * 1024, OFF_O + (p * 4 + s4) * 20480 + kh * 10240 + ((i + 4) >> 2) * 4096);
        SCHED();
      }
    }
  }
  MFMA_DONE();
  if (kh == 0) {
    *(LAS f32x4*)(L + L_RED + (s4 * 5 + 2) * 1024 + l16) = acc2[2];
    *(LAS f32x4*)(L + L_RED + (s4 * 5 + 3) * 1024 + l16) = acc2[3];
  } else {
    *(LAS f32x4*)(L + L_RED + (s4 * 5 + 0) * 1024 + l16) = acc2[0];
    *(LAS f32x4*)(L + L_RED + (s4 * 5 + 1) * 1024 + l16) = acc2[1];
    *(LAS f32x4*)(L + L_RED + (s4 * 5 + 4) * 1024 + l16) = acc2[4];
  }
  bar();
  STAMP(9);
  v4u x1a; v2u x1b = {0, 0};
  {
    const int c0 = kh == 0 ? 0 : 2;
    const f32x4 u0 = (kh == 0 ? acc2[0] : acc2[2]) + ldsf4(L, L_RED + (s4 * 5 + c0) * 1024 + l16);
    const f32x4 u1 = (kh == 0 ? acc2[1] : acc2[3]) + ldsf4(L, L_RED + (s4 * 5 + c0 + 1) * 1024 + l16);
    const f32x4 g0 = ldsf4(L, L_SM + fo * 4), g1 = ldsf4(L, L_SM + fo * 4 + 16);
    x1a.x = pk(fma2(g0.xy, u0.xy, unpk(xa.x)));
    x1a.y = pk(fma2(g0.zw, u0.zw, unpk(xa.y)));
    x1a.z = pk(fma2(g1.xy, u1.xy, unpk(xa.z)));
    x1a.w = pk(fma2(g1.zw, u1.zw, unpk(xa.w)));
    const u32 xco = OFF_XC + (p * 4 + s4) * 20480 + t16 * 16;
    const f32x4 cf0 = ldsf4(L, L_CF + fo * 4), cf1 = ldsf4(L, L_CF + fo * 4 + 16);
    const v4u cpk = lds128(L, L_CPK + (80 * g + fo) * 2), spk = lds128(L, L_SPK + (80 * g + fo) * 2);
    const v4u xc = {pk(unpk(x1a.x) * cf0.xy), pk(unpk(x1a.y) * cf0.zw), pk(unpk(x1a.z) * cf1.xy), pk(unpk(x1a.w) * cf1.zw)};
    { const int ch = 10 * g + (fo >> 3); stw(rs, xc, (ch >> 2) * 1024 + (ch & 3) * 256, xco); }
    const u32 one2 = 0x3f803f80u;
    float S1 = 0.f, S2 = 0.f, T1 = 0.f, T2 = 0.f, T3 = 0.f, T4 = 0.f;
#pragma unroll
    for (int e = 0; e < 4; ++e) {
      S1 = dot2(x1a[e], one2, S1); S2 = dot2(x1a[e], x1a[e], S2); T1 = dot2(xc[e], one2, T1);
      T2 = dot2(xc[e], xc[e], T2); T3 = dot2(xc[e], cpk[e], T3); T4 = dot2(xc[e], spk[e], T4);
    }
    if (kh == 0) {
      const f32x4 u2 = acc2[4] + ldsf4(L, L_RED + (s4 * 5 + 4) * 1024 + l16);
      const f32x4 g2 = ldsf4(L, L_SM + (64 + 4 * g4) * 4);
      x1b.x = pk(fma2(g2.xy, u2.xy, unpk(xb.x)));
      x1b.y = pk(fma2(g2.zw, u2.zw, unpk(xb.y)));
      const f32x4 cf2 = ldsf4(L, L_CF + (64 + 4 * g4) * 4);
      const v2u cp2 = lds64(L, L_CPK + (80 * g + 64 + 4 * g4) * 2), sp2 = lds64(L, L_SPK + (80 * g + 64 + 4 * g4) * 2);
      const v2u xc2 = {pk(unpk(x1b.x) * cf2.xy), pk(unpk(x1b.y) * cf2.zw)};
      const int ch = 10 * g + 8 + (g4 >> 1);
      __builtin_amdgcn_raw_buffer_store_b64(xc2, rs, xco + (ch >> 2) * 1024 + (ch & 3) * 256 + (g4 & 1) * 8, 0, SC1);
#pragma unroll
      for (int e = 0; e < 2; ++e) {
        S1 = dot2(x1b[e], one2, S1); S2 = dot2(x1b[e], x1b[e], S2); T1 = dot2(xc2[e], one2, T1);
        T2 = dot2(xc2[e], xc2[e], T2); T3 = dot2(xc2[e], cp2[e], T3); T4 = dot2(xc2[e], sp2[e], T4);
      }
    }
    const float m0 = x32_sum(swap16_add(S1, S2)), m1 = x32_sum(swap16_add(T1, T2)), m2 = x32_sum(swap16_add(T3, T4));
    if (g4 < 2) stw(rs, __builtin_bit_cast(v4u, f32x4{m0, m1, m2, 0.f}), ((2 * g + kh) * 2 + g4) * 1024 + (16 * s4 + t16) * 16, OFF_MOM + p * 32768);
  }
  drain();
  bar();
  if (tid == 0) publish(ws, OFF_F2 + (p * 8 + g) * 128, T);
  if (tid == 64 && b < 108) publish(ws, OFF_FU + b * 128, T);
  STAMP(10);

  const int j0 = g < 7 ? 7 * g : 48, nk = g < 6 ? 7 : 6;
  const bool act3 = w < nk;
  const int jb = j0 + (act3 ? w : 0);
  constexpr int R3 = 6;
  v4u w3[R3][2];
  const u32 w1o = OFF_W1 + jb * 40960;
  v4u b1v = {0, 0, 0, 0};
  if (act3) {
#pragma unroll
    for (int j = 0; j < R3; ++j) {
      w3[j][0] = ldw(rs, l16 + (j & 1) * 2048, w1o + (j >> 1) * 4096); w3[j][1] = ldw(rs, l16 + (j & 1) * 2048 + 1024, w1o + (j >> 1) * 4096);
      SCHED();
      if (j == 2) VMCNT(4);
    }
    b1v = *(const GAS v4u*)(b1 + (32 * jb + 8 * g4) * 2);
  }
  SCHED();
  if (w == 7) poll(ws, OFF_F2 + p * 8 * 128, 8, lane, T);
  bar();
  STAMP(11);
#pragma unroll
  for (int i = 0; i < 3; ++i) {
    const int gi = w + 8 * i;
    if (gi < 20) dma<4>(rs, l16, OFF_XC + p * 81920 + gi * 4096, L_A + gi * 4096);
  }
  VMCNT(0);
  bar();
  STAMP(12);
  const u32 w2g = OFF_W2 + g * 54 * 5120;
  f32x4 acc3[4][2];
#pragma unroll
  for (int i = 0; i < 4; ++i) { acc3[i][0] = f32x4{0.f, 0.f, 0.f, 0.f}; acc3[i][1] = f32x4{0.f, 0.f, 0.f, 0.f}; }
  if (act3) {
    const int rdA = L_A + l16;
    auto rd = [&](int tt, int j) { return lds128(L, rdA + tt * 20480 + j * 1024); };
    v4u af[2][4];
#pragma unroll
    for (int tt = 0; tt < 4; ++tt) af[0][tt] = rd(tt, 0);
    SCHED();
#pragma unroll
    for (int j = 0; j < 20; ++j) {
      const int cu = j & 1, nx = cu ^ 1;
      const v4u wa = w3[j % R3][0], wb = w3[j % R3][1];
#pragma unroll
      for (int tt = 0; tt < 4; ++tt) {
        mfma_acc(acc3[tt][0], wa, af[cu][tt]);
        if (j + 1 < 20) af[nx][tt] = rd(tt, j + 1);
        SCHED();
        mfma_acc(acc3[tt][1], wb, af[cu][tt]);
        if (j + R3 < 20 && tt == 0 && !ABL_NOFILL3) w3[j % R3][0] = ldw(rs, l16 + ((j + R3) & 1) * 2048, w1o + ((j + R3) >> 1) * 4096);
        if (j + R3 < 20 && tt == 2 && !ABL_NOFILL3) w3[j % R3][1] = ldw(rs, l16 + ((j + R3) & 1) * 2048 + 1024, w1o + ((j + R3) >> 1) * 4096);
        SCHED();
      }
    }
  } else if (w == 7) {
    v4u uv[3], mo[32];
#pragma unroll
    for (int i = 0; i < 32; ++i) { mo[i] = ldw(rs, l16 + (i & 3) * 1024, OFF_MOM + p * 32768 + (i >> 2) * 4096); SCHED(); }
    poll(ws, OFF_FU + 2 * j0 * 128, 2 * nk, lane, T);
#pragma unroll
    for (int i = 0; i < 3; ++i) { uv[i] = ldw(rs, (i * 64 + lane < 24 * nk ? i * 64 + lane : 0) * 16, OFF_UVZ + j0 * 384); SCHED(); }
    float q5[5] = {0.f, 0.f, 0.f, 0.f, 0.f};
    {
      const u32 one2 = 0x3f803f80u;
      const int lo = (lane < 40 ? lane : 0) * 32;
#pragma unroll
      for (int hh = 0; hh < 2; ++hh) {
        const v4u cp = lds128(L, L_CPK + lo + hh * 16), sp = lds128(L, L_SPK + lo + hh * 16);
#pragma unroll
        for (int e = 0; e < 4; ++e) {
          q5[0] = dot2(cp[e], one2, q5[0]); q5[1] = dot2(cp[e], cp[e], q5[1]); q5[2] = dot2(cp[e], sp[e], q5[2]);
          q5[3] = dot2(sp[e], one2, q5[3]); q5[4] = dot2(sp[e], sp[e], q5[4]);
        }
      }
#pragma unroll
      for (int k = 0; k < 5; ++k) q5[k] = x32_sum(x16_sum(row16_sum(lane < 40 ? q5[k] : 0.f)));
    }
    f32x4 ma = {0.f, 0.f, 0.f, 0.f}, mb = {0.f, 0.f, 0.f, 0.f};
#pragma unroll
    for (int i = 0; i < 32; i += 2) { ma += __builtin_bit_cast(f32x4, mo[i]); mb += __builtin_bit_cast(f32x4, mo[i + 1]); }
    const float S1 = ma.x, T1 = ma.y, T3 = ma.z, S2 = mb.x, T2 = mb.y, T4 = mb.z;
    const float iD = 1.f / 640.f;
    const float mean = S1 * iD;
    const float r1 = __builtin_amdgcn_rsqf(fmaxf(S2 * iD - mean * mean, 0.f) + 1e-5f);
    const float H1 = r1 * (T1 - mean * q5[0]) + q5[3];
    const float H2 = r1 * r1 * (T2 - 2.f * mean * T3 + mean * mean * q5[1]) + 2.f * r1 * (T4 - mean * q5[2]) + q5[4];
    const float m2 = H1 * iD;
    const float r2 = __builtin_amdgcn_rsqf(fmaxf(H2 * iD - m2 * m2, 0.f) + 1e-5f);
    const float al = r1 * r2;
    *(LAS f32x4*)(L + L_ROWC + lane * 16) = f32x4{al, -mean * al, r2, -m2 * r2};
#pragma unroll
    for (int i = 0; i < 3; ++i) if (i * 64 + lane < 168) *(LAS v4u*)(L + L_UVZ + (i * 64 + lane) * 16) = uv[i];
    STAMPW(28, 7);
  }
  MFMA_DONE();
  STAMPW(29, 0);
  bar();
  STAMP(13);
  if (act3) {
    const f32x2 bb[4] = {unpk(b1v.x), unpk(b1v.y), unpk(b1v.z), unpk(b1v.w)};
    const int uo = L_UVZ + w * 384 + g4 * 32;
    const f32x4 u0 = ldsf4(L, uo), u1 = ldsf4(L, uo + 16), v0 = ldsf4(L, uo + 128), v1 = ldsf4(L, uo + 144), z0 = ldsf4(L, uo + 256), z1 = ldsf4(L, uo + 272);
    const f32x2 uu[4] = {u0.xy, u0.zw, u1.xy, u1.zw}, vv[4] = {v0.xy, v0.zw, v1.xy, v1.zw}, zz[4] = {z0.xy, z0.zw, z1.xy, z1.zw};
    const float k1c = -2.f * 0.7978845608028654f * 1.4426950408889634f, k3c = k1c * 0.044715f;
#pragma unroll
    for (int tt = 0; tt < 4; ++tt) {
      const f32x4 rc = ldsf4(L, L_ROWC + (16 * tt + t16) * 16);
      const f32x2 al = {rc.x, rc.x}, be = {rc.y, rc.y}, r2 = {rc.z, rc.z}, ga2 = {rc.w, rc.w};
      v4u o;
#pragma unroll
      for (int e = 0; e < 4; ++e) {
        const f32x2 a2 = (e & 1) ? acc3[tt][e >> 1].zw : acc3[tt][e >> 1].xy;
        const f32x2 v = fma2(al, a2, fma2(be, uu[e], fma2(r2, vv[e], fma2(ga2, zz[e], bb[e]))));
        const f32x2 z = v * fma2(v * v, f32x2{k3c, k3c}, f32x2{k1c, k1c});
        const f32x2 d = f32x2{__builtin_amdgcn_exp2f(z.x), __builtin_amdgcn_exp2f(z.y)} + 1.f;
        o[e] = pk(v * f32x2{__builtin_amdgcn_rcpf(d.x), __builtin_amdgcn_rcpf(d.y)});
      }
      stw(rs, o, l16, OFF_HH + ((p * 4 + tt) * 54 + jb) * 1024);
    }
  } else if (w == 7) {
#pragma unroll 1
    for (int i = 0; i < 18; ++i) dma<4>(rs, l16, w2g + 4 * i * 1024, i * 4096);
    dma<3>(rs, l16, w2g + 72 * 1024, 72 * 1024);
    dma<4>(rs, l16, w2g + 27 * 5 * 1024, 76800);
    dma<1>(rs, l16, w2g + (27 * 5 + 4) * 1024, 76800 + 4096);
  }
  if (act3) drain();
  bar();
  if (tid == 0) publish(ws, OFF_F3 + (p * 8 + g) * 128, T);
  STAMP(14);

  const u32 w2o = w2g + 27 * kh * 5120;
  const u32 ring = kh * 76800;
  if (w < 7) {
#pragma unroll
    for (int i = 0; i < 3; ++i) {
      const int gi = w + 7 * i;
      if (gi < 17) dma<4>(rs, l16, w2g + (27 * 5 + 5 + 4 * gi) * 1024, 81920 + gi * 4096);
      else if (gi == 17) dma<2>(rs, l16, w2g + (27 * 5 + 5 + 68) * 1024, 81920 + 68 * 1024);
      if (i == 1) VMCNT(4);
    }
    VMCNT(0);
  } else {
    poll(ws, OFF_F3 + p * 8 * 128, 8, lane, T);
  }
  bar();
  STAMP(15);
  constexpr int RA4 = 15;
  v4u ha[27];
  const u32 hho = OFF_HH + ((p * 4 + s4) * 54 + 27 * kh) * 1024;
#pragma unroll
  for (int i = 0; i < RA4; ++i) {
    if (i > 0 && i % 3 == 0) asm volatile("s_barrier" ::: "memory");
    ha[i] = ldw(rs, l16 + (i & 3) * 1024, hho + (i >> 2) * 4096); SCHED();
  }
  SCHED();
  f32x4 acc4[5];
#pragma unroll
  for (int i = 0; i < 5; ++i) acc4[i] = f32x4{0.f, 0.f, 0.f, 0.f};
  {
    v4u wf[2][5];
    const int rb0 = ring + l16, rb1 = ring + 61440 + l16;
#pragma unroll
    for (int ct = 0; ct < 5; ++ct) wf[0][ct] = lds128(L, rb0 + ct * 1024);
    SCHED();
#pragma unroll
    for (int i = 0; i < 27; ++i) {
      const int n = i / 3;
#pragma unroll
      for (int ct = 0; ct < 5; ++ct) {
        mfma_acc(acc4[ct], wf[i & 1][ct], ha[i]);
        if (i + 1 < 27) wf[(i + 1) & 1][ct] = lds128(L, (((i + 1) % 15) < 12 ? rb0 : rb1) + (((i + 1) % 15) % 12 * 5 + ct) * 1024);
        if (ct == 1 && i + RA4 < 27) ha[i + RA4] = ldw(rs, l16 + ((i + RA4) & 3) * 1024, hho + ((i + RA4) >> 2) * 4096);
        SCHED();
      }
      if (i % 3 == 2) {
        if (n == 3) VMCNT(15);
        if (n == 4) VMCNT(12);
        if (n == 5) VMCNT(6);
        if (n == 6) VMCNT(0);
        bar();
        if (n < 4 && !ABL_NOREFILL4) {
          if (s4 < 3) dma<4>(rs, l16, w2o + (75 + 15 * n + 4 * s4) * 1024, ring + (15 * n + 4 * s4) * 1024);
          else dma<3>(rs, l16, w2o + (75 + 15 * n + 12) * 1024, ring + (15 * n + 12) * 1024);
        }
      }
    }
  }
  MFMA_DONE();
  STAMP(16);
  if (kh == 0) {
    *(LAS f32x4*)(L + L_RED4 + (s4 * 5 + 2) * 1024 + l16) = acc4[2];
    *(LAS f32x4*)(L + L_RED4 + (s4 * 5 + 3) * 1024 + l16) = acc4[3];
  } else {
    *(LAS f32x4*)(L + L_RED4 + (s4 * 5 + 0) * 1024 + l16) = acc4[0];
    *(LAS f32x4*)(L + L_RED4 + (s4 * 5 + 1) * 1024 + l16) = acc4[1];
    *(LAS f32x4*)(L + L_RED4 + (s4 * 5 + 4) * 1024 + l16) = acc4[4];
  }
  bar();
  {
    const int c0 = kh == 0 ? 0 : 2;
    const f32x4 u0 = (kh == 0 ? acc4[0] : acc4[2]) + ldsf4(L, L_RED4 + (s4 * 5 + c0) * 1024 + l16);
    const f32x4 u1 = (kh == 0 ? acc4[1] : acc4[3]) + ldsf4(L, L_RED4 + (s4 * 5 + c0 + 1) * 1024 + l16);
    const f32x4 g0 = ldsf4(L, L_SM + 320 + fo * 4), g1 = ldsf4(L, L_SM + 320 + fo * 4 + 16);
    const f32x4 e0 = ldsf4(L, L_SM + 640 + fo * 4), e1 = ldsf4(L, L_SM + 640 + fo * 4 + 16);
    v4u o;
    o.x = pk(fma2(g0.xy, u0.xy + e0.xy, unpk(x1a.x)));
    o.y = pk(fma2(g0.zw, u0.zw + e0.zw, unpk(x1a.y)));
    o.z = pk(fma2(g1.xy, u1.xy + e1.xy, unpk(x1a.z)));
    o.w = pk(fma2(g1.zw, u1.zw + e1.zw, unpk(x1a.w)));
    *(GAS v4u*)(out + (u32)(tokg * 1280 + (80 * g + fo) * 2)) = o;
    if (kh == 0) {
      const f32x4 u2 = acc4[4] + ldsf4(L, L_RED4 + (s4 * 5 + 4) * 1024 + l16);
      const f32x4 g2 = ldsf4(L, L_SM + 320 + (64 + 4 * g4) * 4), e2 = ldsf4(L, L_SM + 640 + (64 + 4 * g4) * 4);
      const v2u o2 = {pk(fma2(g2.xy, u2.xy + e2.xy, unpk(x1b.x))), pk(fma2(g2.zw, u2.zw + e2.zw, unpk(x1b.y)))};
      *(GAS v2u*)(out + (u32)(tokg * 1280 + (80 * g + 64 + 4 * g4) * 2)) = o2;
    }
  }
  STAMP(17);
  asm volatile("fused_pf_end:" ::: "memory");
}

extern "C" void kernel_launch(void* const* d_in, const int* in_sizes, int n_in, void* d_out, int out_size, void* d_ws, size_t ws_size,
                              hipStream_t stream) {
  (void)in_sizes; (void)n_in; (void)out_size; (void)ws_size;
  (void)hipFuncSetAttribute((const void*)fused_block, hipFuncAttributeMaxDynamicSharedMemorySize, LDS_BYTES);
  fused_block<<<NWG, NTHR, LDS_BYTES, stream>>>((const u16*)d_in[0], (const u16*)d_in[1], (const u16*)d_in[2], (const u16*)d_in[4],
                                                (const u16*)d_in[5], (const u16*)d_in[6], (const u16*)d_in[7], (const u16*)d_in[8],
                                                (const u16*)d_in[9], (u16*)d_out, (unsigned char*)d_ws);
}
```

```cpp
#include <hip/hip_runtime.h>

typedef unsigned short u16;
typedef unsigned int u32;
typedef unsigned long long u64;
typedef u32 v4u __attribute__((ext_vector_type(4)));
typedef u32 v2u __attribute__((ext_vector_type(2)));
typedef float f32x4 __attribute__((ext_vector_type(4)));
typedef float f32x2 __attribute__((ext_vector_type(2)));
typedef short s16x8 __attribute__((ext_vector_type(8)));
typedef short s16x4 __attribute__((ext_vector_type(4)));
typedef __bf16 bf16x2 __attribute__((ext_vector_type(2)));
#define GAS __attribute__((address_space(1)))
#define LAS __attribute__((address_space(3)))
#define DI __device__ __forceinline__
typedef __amdgpu_buffer_rsrc_t rsrc_t;


constexpr int NWG = 256, NTHR = 512;
constexpr u32 OFF_E = 0, OFF_F0 = 0x1000, OFF_F1 = 0xA000, OFF_F2 = 0x12000, OFF_F3 = 0x1A000;
constexpr u32 OFF_MODP = 0x30000, OFF_WQ = 0x40000, OFF_WO = 0x300000, OFF_W1 = 0x400000, OFF_W2 = 0x700000;
constexpr u32 OFF_O = 0xA00000, OFF_XC = 0xD00000, OFF_HH = 0x1000000, OFF_MOM = 0x16C0000, OFF_UVZ = 0x17C0000, OFF_STAT = 0x17C8000, WS_USED = 0x1800000;
constexpr u32 OFF_FU = 0x22000, OFF_F0B = 0x26000;
constexpr int L_A = 0, L_OB = 39424, L_WO = 51200, L_JOB = 102400, L_GV = 143360, L_CNT = 145408, L_RED = 0, L_RED4 = 102400, L_SM = 153600, L_VEC = 154624, LDS_BYTES = 163840;
constexpr int L_SPK = 154624, L_CPK = 155904, L_ONE = 157184, L_CF = 157312, L_ROWC = 157696, L_UVZ = 158720, L_STAT = 161280;
constexpr int QS = 488;
constexpr int OS = 176;
constexpr int SPIN_CAP = 1 << 14;
constexpr int SC1 = 16;

DI f32x4 mfma(v4u a, v4u b, f32x4 c) {
  return __builtin_amdgcn_mfma_f32_16x16x32_bf16(__builtin_bit_cast(s16x8, a), __builtin_bit_cast(s16x8, b), c, 0, 0, 0);
}
DI void mfma_acc(f32x4& c, v4u a, v4u b) { c = mfma(a, b, c); }
DI u32 pack2(float a, float b) { f32x2 v = {a, b}; return __builtin_bit_cast(u32, __builtin_convertvector(v, bf16x2)); }
DI float blo(u32 u) { return __builtin_bit_cast(float, u << 16); }
DI float bhi(u32 u) { return __builtin_bit_cast(float, u & 0xffff0000u); }
DI f32x2 unpk(u32 u) { return f32x2{blo(u), bhi(u)}; }
DI f32x2 fma2(f32x2 a, f32x2 b, f32x2 c) { return __builtin_elementwise_fma(a, b, c); }
DI u32 pk(f32x2 v) { return __builtin_bit_cast(u32, __builtin_convertvector(v, bf16x2)); }
DI float dot2(u32 a, u32 b, float c) { return __builtin_amdgcn_fdot2_f32_bf16(__builtin_bit_cast(bf16x2, a), __builtin_bit_cast(bf16x2, b), c, false); }
DI float fma1(float a, float b, float c) { float r; asm("v_fma_f32 %0, %1, %2, %3" : "=v"(r) : "v"(a), "v"(b), "v"(c)); return r; }
DI float bf2f(u16 v) { return __builtin_bit_cast(float, (u32)v << 16); }
template <int CTRL> DI float dpp_add(float v) {
  return v + __builtin_bit_cast(float, __builtin_amdgcn_update_dpp(0, __builtin_bit_cast(int, v), CTRL, 0xf, 0xf, true));
}
DI float row16_sum(float v) { v = dpp_add<0x128>(v); v = dpp_add<0x124>(v); v = dpp_add<0x122>(v); return dpp_add<0x121>(v); }
DI float max3(float a, float b, float c) { float r; asm("v_max3_f32 %0, %1, %2, %3" : "=v"(r) : "v"(a), "v"(b), "v"(c)); return r; }
DI float max2(float a, float b) { float r; asm("v_max_f32 %0, %1, %2" : "=v"(r) : "v"(a), "v"(b)); return r; }
DI float x16_sum(float v) {
  auto r = __builtin_amdgcn_permlane16_swap(__builtin_bit_cast(u32, v), __builtin_bit_cast(u32, v), false, false);
  const u32 a = r[0], c = r[1];
  return __builtin_bit_cast(float, a) + __builtin_bit_cast(float, c);
}
DI float x32_sum(float v) {
  auto r = __builtin_amdgcn_permlane32_swap(__builtin_bit_cast(u32, v), __builtin_bit_cast(u32, v), false, false);
  const u32 a = r[0], c = r[1];
  return __builtin_bit_cast(float, a) + __builtin_bit_cast(float, c);
}
DI float x16_max(float v) {
  auto r = __builtin_amdgcn_permlane16_swap(__builtin_bit_cast(u32, v), __builtin_bit_cast(u32, v), false, false);
  const u32 a = r[0], c = r[1];
  return max2(__builtin_bit_cast(float, a), __builtin_bit_cast(float, c));
}
DI float x32_max(float v) {
  auto r = __builtin_amdgcn_permlane32_swap(__builtin_bit_cast(u32, v), __builtin_bit_cast(u32, v), false, false);
  const u32 a = r[0], c = r[1];
  return max2(__builtin_bit_cast(float, a), __builtin_bit_cast(float, c));
}
DI float swap16_add(float a, float b) {
  auto r = __builtin_amdgcn_permlane16_swap(__builtin_bit_cast(u32, a), __builtin_bit_cast(u32, b), false, false);
  const u32 x = r[0], y = r[1];
  return __builtin_bit_cast(float, x) + __builtin_bit_cast(float, y);
}
DI float swap32_add(float a, float b) {
  auto r = __builtin_amdgcn_permlane32_swap(__builtin_bit_cast(u32, a), __builtin_bit_cast(u32, b), false, false);
  const u32 x = r[0], y = r[1];
  return __builtin_bit_cast(float, x) + __builtin_bit_cast(float, y);
}
template <int V> struct IntC { static constexpr int value = V; };
template <bool V> struct BoolC { static constexpr bool value = V; };
template <int N, int I = 0, class F> DI void static_for(F&& f) {
  if constexpr (I < N) { f(IntC<I>{}); static_for<N, I + 1>(f); }
}
DI void bar() { asm volatile("s_waitcnt lgkmcnt(0)\n\ts_barrier" ::: "memory"); }
DI void drain() { asm volatile("s_waitcnt vmcnt(0)" ::: "memory"); }
#define SCHED() __builtin_amdgcn_sched_barrier(0)
#define VMCNT(n) asm volatile("s_waitcnt vmcnt(" #n ")" ::: "memory")
DI v4u ldw(rsrc_t rs, u32 voff, u32 soff) { return __builtin_amdgcn_raw_buffer_load_b128(rs, voff, soff, SC1); }
DI void stw(rsrc_t rs, v4u v, u32 voff, u32 soff) { __builtin_amdgcn_raw_buffer_store_b128(v, rs, voff + soff, 0, SC1); }
DI v4u lds128(LAS unsigned char* L, int off) { return *(LAS v4u*)(L + off); }
DI f32x4 ldsf4(LAS unsigned char* L, int off) { return *(LAS f32x4*)(L + off); }
DI v2u lds64(LAS unsigned char* L, int off) { return *(LAS v2u*)(L + off); }
DI v2u ldstr(LAS unsigned char* L, int off) {
  return __builtin_bit_cast(v2u, __builtin_amdgcn_ds_read_tr16_b64_v4i16((LAS s16x4*)(L + off)));
}
template <int N> DI void dma(rsrc_t rs, u32 voff, u32 soff, u32 ldsoff) {
#define DMA_I(o) "buffer_load_dwordx4 %1, %2, %3 offen offset:" #o " sc1 lds\n\t"
#define DMA_ARGS :: "s"(ldsoff), "v"(voff), "s"(rs), "s"(soff) : "memory"
  if constexpr (N == 4) asm volatile("s_mov_b32 m0, %0\n\ts_nop 0\n\t" DMA_I(0) DMA_I(1024) DMA_I(2048) DMA_I(3072) DMA_ARGS);
  else if constexpr (N == 3) asm volatile("s_mov_b32 m0, %0\n\ts_nop 0\n\t" DMA_I(0) DMA_I(1024) DMA_I(2048) DMA_ARGS);
  else if constexpr (N == 2) asm volatile("s_mov_b32 m0, %0\n\ts_nop 0\n\t" DMA_I(0) DMA_I(1024) DMA_ARGS);
  else asm volatile("s_mov_b32 m0, %0\n\ts_nop 0\n\t" DMA_I(0) DMA_ARGS);
}
DI void poll(unsigned char* ws, u32 off, int n, int lane, u64 T) {
  const GAS u64* f = (const GAS u64*)(ws + off + (lane < n ? lane : 0) * 128);
#pragma unroll 1
  for (int spins = 0; spins < SPIN_CAP; ++spins) {
    const u64 v = __hip_atomic_load(f, __ATOMIC_RELAXED, __HIP_MEMORY_SCOPE_AGENT);
    if (__all(v == T)) break;
  }
  __builtin_amdgcn_fence(__ATOMIC_ACQUIRE, "wavefront");
}
DI void poll256(unsigned char* ws, u32 off, int lane, u64 T) {
  const GAS u64* f = (const GAS u64*)(ws + off + lane * 32);
#pragma unroll 1
  for (int spins = 0; spins < SPIN_CAP; ++spins) {
    const u64 v0 = __hip_atomic_load(f, __ATOMIC_RELAXED, __HIP_MEMORY_SCOPE_AGENT);
    const u64 v1 = __hip_atomic_load(f + 256, __ATOMIC_RELAXED, __HIP_MEMORY_SCOPE_AGENT);
    const u64 v2 = __hip_atomic_load(f + 512, __ATOMIC_RELAXED, __HIP_MEMORY_SCOPE_AGENT);
    const u64 v3 = __hip_atomic_load(f + 768, __ATOMIC_RELAXED, __HIP_MEMORY_SCOPE_AGENT);
    if (__all(v0 == T && v1 == T && v2 == T && v3 == T)) break;
  }
  __builtin_amdgcn_fence(__ATOMIC_ACQUIRE, "wavefront");
}
DI void publish(unsigned char* ws, u32 off, u64 T) {
  __hip_atomic_store((GAS u64*)(ws + off), T, __ATOMIC_RELAXED, __HIP_MEMORY_SCOPE_AGENT);
}
#define PHASE() asm volatile("" ::: "memory")

typedef const GAS unsigned char* gptr;
__global__ __launch_bounds__(NTHR, 2) void fused_block(const u16* a_x, const u16* a_wqkv, const u16* a_wo, const u16* a_w1, const u16* a_b1,
                                                       const u16* a_w2, const u16* a_b2, const u16* a_c, const u16* a_wmod, u16* a_out,
                                                       unsigned char* a_ws) {
  extern __shared__ __attribute__((aligned(16))) unsigned char smem[];
  const __attribute__((address_space(4))) u64* ka = (const __attribute__((address_space(4))) u64*)__builtin_amdgcn_kernarg_segment_ptr();
  u64 k0 = ka[0], k1 = ka[1], k2 = ka[2], k3 = ka[3], k4 = ka[4], k5 = ka[5], k6 = ka[6], k7 = ka[7], k8 = ka[8], k9 = ka[9], k10 = ka[10];
  asm volatile("" : "+s"(k0), "+s"(k1), "+s"(k2), "+s"(k3), "+s"(k4), "+s"(k5), "+s"(k6), "+s"(k7), "+s"(k8), "+s"(k9), "+s"(k10));
  const gptr x = (gptr)k0, wqkv = (gptr)k1, wo = (gptr)k2, w1 = (gptr)k3, b1 = (gptr)k4, w2 = (gptr)k5, b2 = (gptr)k6, cvec = (gptr)k7, wmod = (gptr)k8;
  GAS unsigned char* const out = (GAS unsigned char*)k9;
  unsigned char* const ws = (unsigned char*)k10;
  LAS unsigned char* L = (LAS unsigned char*)smem;
  const int tid = threadIdx.x, lane = tid & 63, b = blockIdx.x;
  const int w = __builtin_amdgcn_readfirstlane(tid >> 6);
  const int xcd = b & 7, jj = b >> 3;
  const int p = (xcd >> 1) * 8 + (jj >> 2), g = (xcd & 1) * 4 + (jj & 3);
  const rsrc_t rs = __builtin_amdgcn_make_buffer_rsrc(ws, 0, WS_USED, 0x00020000);
  const int g4 = lane >> 4, t16 = lane & 15;
  const u32 l16 = lane * 16;
  PHASE();

  u64 Ev = 0;
  if (w == 3) Ev = __hip_atomic_load((const GAS u64*)(ws + OFF_E), __ATOMIC_RELAXED, __HIP_MEMORY_SCOPE_AGENT);
  if (tid == 0) { *(LAS u32*)(L + L_CNT) = 0u; *(LAS u32*)(L + L_CNT + 4) = 0u; }
  bar();
  const int id2 = (w - 4) * 256 + b;
  const bool hasjob = w < 4 ? w * 256 + b < 640 : id2 < 972;
  const int job = w < 4 ? (hasjob ? w * 256 + b : 0) : (id2 < 540 ? 1072 + id2 : (hasjob ? 640 + id2 - 540 : 1072));
  gptr jsrc; u32 jstride, jdst, jA, jB; int ppr; bool paired;
  if (job < 480) {
    const int gg = job / 60, rem = job % 60, part = rem / 20, j = rem % 20;
    jsrc = wqkv + (u32)(32 * j * 3840 + part * 1280 + gg * 160);
    jstride = 3840; ppr = 10; paired = false;
    jdst = OFF_WQ + (gg * 15 + part * 5) * 20480 + j * 1024; jA = 40960; jB = 20480;
  } else if (job < 640) {
    const int id = job - 480, gg = id / 20, j = id % 20;
    jsrc = wo + (u32)(32 * j * 1280 + gg * 160);
    jstride = 1280; ppr = 10; paired = true;
    jdst = OFF_WO + (gg * 20 + j) * 5120; jA = 2048; jB = 1024;
  } else if (job < 1072) {
    const int id = job - 640, gg = id / 54, j = id % 54;
    jsrc = w2 + (u32)(32 * j * 1280 + gg * 160);
    jstride = 1280; ppr = 10; paired = true;
    jdst = OFF_W2 + (gg * 54 + j) * 5120; jA = 2048; jB = 1024;
  } else {
    const int id = job - 1072, jb2 = id / 20, j = id % 20;
    jsrc = w1 + (u32)(32 * j * 3456 + jb2 * 128);
    jstride = 3456; ppr = 8; paired = true;
    jdst = OFF_W1 + (2 * jb2 * 20 + j) * 2048; jA = 40960; jB = 1024;
  }
    const int cl = b % 60, ks = b < 240 ? b / 60 : 3;
  v4u jr[5];
  auto job_loads = [&]() {
    const u32 magic = ppr == 10 ? 6554u : 8192u;
#pragma unroll
    for (int it = 0; it < 5; ++it) {
      u32 idx = it * 64 + lane;
      if (it == 4 && ppr == 8) idx = lane;
      const u32 row = (idx * magic) >> 16, pc = idx - row * ppr;
      jr[it] = *(const GAS v4u*)(jsrc + (row * jstride + pc * 16));
      SCHED();
    }
  };
  auto run_job = [&]() {
    LAS unsigned char* sc = L + L_JOB + w * 5120;
#pragma unroll
    for (int it = 0; it < 5; ++it)
      if (it < 4 || ppr == 10) *(LAS v4u*)(sc + it * 1024 + l16) = jr[it];
    __builtin_amdgcn_fence(__ATOMIC_RELEASE, "wavefront");
    __builtin_amdgcn_wave_barrier();
    __builtin_amdgcn_fence(__ATOMIC_ACQUIRE, "wavefront");
    const int rowb = ppr * 16;
    const int q = t16 >> 2, pp = lane & 3;
    const int rbase = (8 * g4 + q) * rowb;
#pragma unroll
    for (int ct = 0; ct < 5; ++ct) {
      if (ct < 4 || ppr == 10) {
        const int cb = (paired && ct < 4) ? 64 * (ct >> 1) + 8 * (ct & 1) + 16 * pp : 32 * ct + 8 * pp;
        const v2u lo = ldstr(sc, rbase + cb), hi = ldstr(sc, rbase + 4 * rowb + cb);
        stw(rs, v4u{lo.x, lo.y, hi.x, hi.y}, l16, jdst + (ct >> 1) * jA + (ct & 1) * jB);
      }
    }
  };
  v4u xr[2][5]; v2u xh[5];
  u32 xo[3];
#pragma unroll
  for (int a = 0; a < 3; ++a) {
    const int tok = 64 * p - 16 + 4 * (a < 2 ? w + 8 * a : (w & 3) + 16) + g4;
    xo[a] = (tok < 0 ? 0 : tok) * 1280 + t16 * 16 + (a == 2 ? (w >> 2) * 8 : 0);
  }
  auto x_chunk = [&](int m) {
    xr[0][m] = *(const GAS v4u*)(x + (xo[0] + m * 256)); SCHED();
    xr[1][m] = *(const GAS v4u*)(x + (xo[1] + m * 256)); SCHED();
    xh[m] = *(const GAS v2u*)(x + (xo[2] + m * 256)); SCHED();
  };
  auto lds_add = [&](int off) { __hip_atomic_fetch_add((LAS u32*)(L + off), 1u, __ATOMIC_RELAXED, __HIP_MEMORY_SCOPE_WORKGROUP); };
  auto lds_wait = [&](int off, u32 n) {
#pragma unroll 1
    for (int spins = 0; spins < SPIN_CAP * 16; ++spins) {
      if (__hip_atomic_load((LAS u32*)(L + off), __ATOMIC_RELAXED, __HIP_MEMORY_SCOPE_WORKGROUP) >= n) break;
      __builtin_amdgcn_s_sleep(1);
    }
    asm volatile("" ::: "memory");
  };
  u32 pf = 0;
  if (w < 4) {
    v4u gw[5];
#pragma unroll
    for (int i = 0; i < 5; ++i) { gw[i] = *(const GAS v4u*)(wmod + (u32)((160 * ks + 40 * w + (lane >> 3) + 8 * i) * 7680 + cl * 128 + (lane & 7) * 16)); SCHED(); }
    const u16 gc = *(const GAS u16*)(cvec + (160 * ks + 40 * w + (lane < 40 ? lane : 0)) * 2);
    SCHED();
    v4u sr[10];
    if (w == 3) {
#pragma unroll
      for (int i = 0; i < 10; ++i) { sr[i] = *(const GAS v4u*)(x + (u32)((8 * b + (lane >> 3)) * 1280 + ((lane & 7) + 8 * i) * 16)); SCHED(); }
    } else {
      job_loads();
    }
    asm volatile("s_barrier" ::: "memory");
    x_chunk(0); x_chunk(1); x_chunk(2);
    PHASE();
    const float c = bf2f(gc);
    const float sl = c * __builtin_amdgcn_rcpf(1.f + __expf(-c));
    f32x2 acc[4];
#pragma unroll
    for (int i = 0; i < 5; ++i) {
      const float sc = __builtin_bit_cast(float, __builtin_amdgcn_ds_bpermute(((lane >> 3) + 8 * i) * 4, __builtin_bit_cast(int, sl)));
      const f32x2 s2 = {sc, sc};
#pragma unroll
      for (int e = 0; e < 4; ++e) acc[e] = i == 0 ? unpk(gw[i][e]) * s2 : fma2(unpk(gw[i][e]), s2, acc[e]);
    }
    float u[8];
#pragma unroll
    for (int e = 0; e < 4; ++e) { u[2 * e] = dpp_add<0x128>(acc[e].x); u[2 * e + 1] = dpp_add<0x128>(acc[e].y); }
    float v[4];
#pragma unroll
    for (int e = 0; e < 4; ++e) v[e] = swap16_add(u[2 * e], u[2 * e + 1]);
    const float z0 = swap32_add(v[0], v[1]), z1 = swap32_add(v[2], v[3]);
    const float z = (lane & 8) ? z1 : z0;
    const int col = (lane & 7) * 8 + ((lane >> 1) & 4) + ((lane >> 4) & 2) + ((lane >> 4) & 1);
    *(LAS float*)(L + L_GV + w * 256 + col * 4) = z;
    asm volatile("s_waitcnt lgkmcnt(0)" ::: "memory");
    if (lane == 0) lds_add(L_CNT);
    PHASE();
    if (w == 3) {
      float s0 = 0.f, s1 = 0.f, q0 = 0.f, q1 = 0.f;
#pragma unroll
      for (int i = 0; i < 10; ++i) {
        s0 = dot2(sr[i].x, 0x3f803f80u, s0); q0 = dot2(sr[i].x, sr[i].x, q0); s1 = dot2(sr[i].y, 0x3f803f80u, s1); q1 = dot2(sr[i].y, sr[i].y, q1);
        s0 = dot2(sr[i].z, 0x3f803f80u, s0); q0 = dot2(sr[i].z, sr[i].z, q0); s1 = dot2(sr[i].w, 0x3f803f80u, s1); q1 = dot2(sr[i].w, sr[i].w, q1);
      }
      const float mean = dpp_add<0x141>(dpp_add<0x4e>(dpp_add<0xb1>(s0 + s1))) * (1.f / 640.f);
      const float var = fmaxf(dpp_add<0x141>(dpp_add<0x4e>(dpp_add<0xb1>(q0 + q1))) * (1.f / 640.f) - mean * mean, 0.f);
      const float rstd = __builtin_amdgcn_rsqf(var + 1e-5f);
      if ((lane & 7) == 0)
        __builtin_amdgcn_raw_buffer_store_b64(__builtin_bit_cast(v2u, f32x2{rstd, -mean * rstd}), rs, (8 * b + (lane >> 3)) * 8, OFF_STAT, SC1);
      lds_wait(L_CNT, 4);
      const float s = (*(LAS float*)(L + L_GV + lane * 4) + *(LAS float*)(L + L_GV + 256 + lane * 4)) +
                      (*(LAS float*)(L + L_GV + 512 + lane * 4) + *(LAS float*)(L + L_GV + 768 + lane * 4));
      if (b < 240) __builtin_amdgcn_raw_buffer_store_b32(__builtin_bit_cast(u32, s), rs, lane * 4, OFF_MODP + (ks * 3840 + cl * 64) * 4, SC1);
    } else if (hasjob) {
      run_job();
    }
    drain();
    if (lane == 0) lds_add(L_CNT + 4);
    if (w == 3) {
      lds_wait(L_CNT + 4, 4);
      u64 e3 = Ev;
      asm volatile("" : "+v"(e3));
      if (lane == 0) publish(ws, OFF_F0 + b * 32, e3 + 1);
    }
  } else {
    if (w == 7) {
      u64 pc; int len;
      asm volatile("fused_pf_begin:\n\ts_getpc_b64 %0\n\ts_mov_b32 %1, fused_pf_end-fused_pf_begin" : "=s"(pc), "=s"(len));
      const int o = (jj * 16 + t16) * 128;
      if (o < len) pf = *(const GAS u32*)(pc + (u32)o);
    }
    asm volatile("s_barrier" ::: "memory");
    x_chunk(0); x_chunk(1); x_chunk(2);
  }
  if (w == 3) {
    u64 e3 = Ev;
    asm volatile("" : "+v"(e3));
    poll256(ws, OFF_F0, lane, e3 + 1);
    if (b == 0 && lane == 0) publish(ws, OFF_E, e3 + 1);
    v4u pv1[5][4]; v2u st[2];
#pragma unroll
    for (int r = 0; r < 5; ++r)
#pragma unroll
      for (int k = 0; k < 4; ++k) { pv1[r][k] = ldw(rs, (r * 64 + lane) * 16, OFF_MODP + k * 15360); SCHED(); }
#pragma unroll
    for (int r = 0; r < 2; ++r) {
      int tok = 64 * p - 16 + r * 64 + (r == 0 || lane < 16 ? lane : 0);
      tok = tok < 0 ? 0 : tok;
      st[r] = __builtin_amdgcn_raw_buffer_load_b64(rs, tok * 8, OFF_STAT, SC1);
    }
    SCHED();
#pragma unroll
    for (int r = 0; r < 5; ++r) {
      const f32x4 s = (__builtin_bit_cast(f32x4, pv1[r][0]) + __builtin_bit_cast(f32x4, pv1[r][1])) + (__builtin_bit_cast(f32x4, pv1[r][2]) + __builtin_bit_cast(f32x4, pv1[r][3]));
      const int qd = r * 64 + lane, col = (qd < 160 ? qd : qd - 160) * 4;
      const int vo = L_VEC + (col >> 3) * 80 + (col & 4) * 4;
      if (qd < 160) *(LAS f32x4*)(L + vo + 32) = s;
      else *(LAS f32x4*)(L + vo) = s + 1.f;
    }
    *(LAS v2u*)(L + L_STAT + lane * 8) = st[0];
    if (lane < 16) *(LAS v2u*)(L + L_STAT + 512 + lane * 8) = st[1];
    if (lane == 0) *(LAS u64*)(L + L_CNT + 8) = e3 + 1;
  }
  PHASE();
  asm volatile("" ::"v"(pf));
  bar();
  const v2u Tv = lds64(L, L_CNT + 8);
  const u64 T = ((u64)__builtin_amdgcn_readfirstlane(Tv.y) << 32) | __builtin_amdgcn_readfirstlane(Tv.x);

  constexpr int R1 = 6;
  v4u wr[R1][2];
  const u32 wq0 = OFF_WQ + (g * 15 + 2 * w) * 20480, wq1 = OFF_WQ + (g * 15 + (w < 7 ? 2 * w + 1 : 14)) * 20480;
#pragma unroll
  for (int j = 0; j < R1; ++j) {
    if (j == 2 || j == 4) asm volatile("s_barrier" ::: "memory");
    wr[j][0] = ldw(rs, l16 + (j & 3) * 1024, wq0 + (j >> 2) * 4096); wr[j][1] = ldw(rs, l16 + (j & 3) * 1024, wq1 + (j >> 2) * 4096);
    SCHED();
  }
  f32x4 acc1[5][2];
#pragma unroll
  for (int i = 0; i < 5; ++i) { acc1[i][0] = f32x4{0.f, 0.f, 0.f, 0.f}; acc1[i][1] = f32x4{0.f, 0.f, 0.f, 0.f}; }
  const int rd_even = g4 * 256 + ((t16 ^ g4) * 16), rd_odd = rd_even ^ 64;
  int wad[3];
  float xrs[3], xnb[3];
#pragma unroll
  for (int a = 0; a < 3; ++a) {
    const int grp = a < 2 ? w + 8 * a : (w & 3) + 16, tt = grp >> 2, t = 4 * (grp & 3) + g4;
    wad[a] = L_A + tt * 20480 + (t16 >> 2) * 1024 + (t16 & 3) * 256 + ((t ^ (t16 & 7)) * 16) + (a == 2 ? (w >> 2) * 8 : 0);
    const v2u st = lds64(L, L_STAT + (4 * grp + g4) * 8);
    xrs[a] = __builtin_bit_cast(float, (u32)st.x); xnb[a] = __builtin_bit_cast(float, (u32)st.y);
  }
  const int vad = L_VEC + t16 * 80, vah = vad + (w >> 2) * 16;
  f32x4 cm[3], sv[3];
  auto ldmod = [&](int m) {
    cm[0] = ldsf4(L, vad + m * 1280); sv[0] = ldsf4(L, vad + m * 1280 + 32);
    cm[1] = ldsf4(L, vad + m * 1280 + 16); sv[1] = ldsf4(L, vad + m * 1280 + 48);
    cm[2] = ldsf4(L, vah + m * 1280); sv[2] = ldsf4(L, vah + m * 1280 + 32);
  };
#pragma unroll
  for (int m = 0; m < 2; ++m) {
    ldmod(m);
#pragma unroll
    for (int a = 0; a < 2; ++a) {
      const f32x2 r2 = {xrs[a], xrs[a]}, n2 = {xnb[a], xnb[a]};
      *(LAS v4u*)(L + wad[a] + m * 4096) =
          v4u{pk(fma2(fma2(unpk(xr[a][m].x), r2, n2), cm[0].xy, sv[0].xy)), pk(fma2(fma2(unpk(xr[a][m].y), r2, n2), cm[0].zw, sv[0].zw)),
              pk(fma2(fma2(unpk(xr[a][m].z), r2, n2), cm[1].xy, sv[1].xy)), pk(fma2(fma2(unpk(xr[a][m].w), r2, n2), cm[1].zw, sv[1].zw))};
    }
    const f32x2 r2 = {xrs[2], xrs[2]}, n2 = {xnb[2], xnb[2]};
    *(LAS v2u*)(L + wad[2] + m * 4096) = v2u{pk(fma2(fma2(unpk(xh[m].x), r2, n2), cm[2].xy, sv[2].xy)), pk(fma2(fma2(unpk(xh[m].y), r2, n2), cm[2].zw, sv[2].zw))};
  }
  bar();
  PHASE();
  v4u pvs[4];
  {
    int rdA[2] = {L_A + rd_even, L_A + rd_odd}, rdB[2] = {L_A + 81920 + rd_even, L_A + 81920 + rd_odd};
    asm volatile("" : "+v"(rdA[0]), "+v"(rdA[1]), "+v"(rdB[0]), "+v"(rdB[1]));
    auto rd = [&](int tt, int j) { return tt < 4 ? lds128(L, rdA[j & 1] + tt * 20480 + j * 1024) : lds128(L, rdB[j & 1] + j * 1024); };
    v4u af[2][5];
#pragma unroll
    for (int tt = 0; tt < 5; ++tt) af[0][tt] = rd(tt, 0);
    SCHED();
    float pu[8]; v4u po;
    auto uop = [&](auto oc, auto mc) {
      constexpr int o = decltype(oc)::value, mp = decltype(mc)::value;
      if constexpr (o < 58) {
        constexpr int a = o / 29, i = o % 29;
        constexpr int kind[29] = {0, 0, 0, 0, 1, 1, 0, 0, 1, 1, 0, 0, 2, 2, 1, 1, 2, 2, 1, 1, 3, 2, 2, 3, 2, 2, 3, 3, 4};
        constexpr int el[29] = {0, 1, 2, 3, 0, 1, 4, 5, 2, 3, 6, 7, 0, 1, 4, 5, 2, 3, 6, 7, 0, 4, 5, 1, 6, 7, 2, 3, 0};
        constexpr int e = el[i];
        if constexpr (kind[i] == 0) pu[e] = (e & 1) ? bhi(xr[a][mp][e >> 1]) : blo(xr[a][mp][e >> 1]);
        else if constexpr (kind[i] == 1) pu[e] = fma1(pu[e], xrs[a], xnb[a]);
        else if constexpr (kind[i] == 2) pu[e] = fma1(pu[e], cm[e >> 2][e & 3], sv[e >> 2][e & 3]);
        else if constexpr (kind[i] == 3) po[e] = pk(f32x2{pu[2 * e], pu[2 * e + 1]});
        else *(LAS v4u*)(L + wad[a] + mp * 4096) = po;
      } else {
        constexpr int i = o - 58;
        constexpr int kind[15] = {0, 0, 0, 0, 1, 1, 1, 1, 2, 2, 2, 2, 3, 3, 4};
        constexpr int el[15] = {0, 1, 2, 3, 0, 1, 2, 3, 0, 1, 2, 3, 0, 1, 0};
        constexpr int e = el[i];
        if constexpr (kind[i] == 0) pu[e] = (e & 1) ? bhi(xh[mp][e >> 1]) : blo(xh[mp][e >> 1]);
        else if constexpr (kind[i] == 1) pu[e] = fma1(pu[e], xrs[2], xnb[2]);
        else if constexpr (kind[i] == 2) pu[e] = fma1(pu[e], cm[2][e], sv[2][e]);
        else if constexpr (kind[i] == 3) po[e] = pk(f32x2{pu[2 * e], pu[2 * e + 1]});
        else *(LAS v2u*)(L + wad[2] + mp * 4096) = v2u{po.x, po.y};
      }
    };
    static_for<20>([&](auto jc) {
      constexpr int j = decltype(jc)::value;
      constexpr int m = j >> 2, q = j & 3, cu = j & 1, nx = cu ^ 1, mp = m + 2 < 5 ? m + 2 : 4;
      constexpr bool more = j + 1 < 20, fill = j + R1 < 20, prep = m < 3;
      if constexpr (j == 1) { if (w >= 4) job_loads(); }
      if constexpr (j == 9) { if (w >= 4 && hasjob) run_job(); }
      if constexpr (j == 16) {
        const u32 vo = 1920 * 4 + (tid < 320 ? tid : 0) * 16;
#pragma unroll
        for (int k = 0; k < 4; ++k) { pvs[k] = ldw(rs, vo, OFF_MODP + k * 15360); SCHED(); }
      }
      const v4u wa = wr[j % R1][0], wb = wr[j % R1][1];
      static_for<10>([&](auto sc) {
        constexpr int sl = decltype(sc)::value, tt = sl >> 1, S = q * 10 + sl;
        mfma_acc(acc1[tt][sl & 1], (sl & 1) ? wb : wa, af[cu][tt]);
        if constexpr (more && !(sl & 1)) af[nx][tt] = rd(tt, j + 1);
        if constexpr (fill && sl == 3) wr[j % R1][0] = ldw(rs, l16 + ((j + R1) & 3) * 1024, wq0 + ((j + R1) >> 2) * 4096);
        if constexpr (fill && sl == 7) wr[j % R1][1] = ldw(rs, l16 + ((j + R1) & 3) * 1024, wq1 + ((j + R1) >> 2) * 4096);
        if constexpr (prep && S == 0) ldmod(mp);
        if constexpr (m < 2 && S == 5) x_chunk(m + 3);
        if constexpr (prep && S >= 2) {
          constexpr int o0 = (S - 2) * 73 / 38, o1 = (S - 1) * 73 / 38;
          static_for<o1 - o0>([&](auto oc) { uop(IntC<o0 + decltype(oc)::value>{}, IntC<mp>{}); });
        }
        SCHED();
      });
      if constexpr (m < 3 && q == 3) bar();
    });
  }
  if (w >= 4) drain();
  bar();
  PHASE();
  if (tid == 256) publish(ws, OFF_F0B + b * 32, T);
  if (tid < 320) {
    f32x4 s = (__builtin_bit_cast(f32x4, pvs[0]) + __builtin_bit_cast(f32x4, pvs[1])) + (__builtin_bit_cast(f32x4, pvs[2]) + __builtin_bit_cast(f32x4, pvs[3]));
    if (tid >= 160) s += 1.f;
    *(LAS v2u*)(L + L_SPK + tid * 8) = v2u{pk(s.xy), pk(s.zw)};
  } else if (tid < 324) {
    *(LAS u32*)(L + L_ONE + (tid - 320) * 4) = 0x3f803f80u;
    *(LAS u32*)(L + L_ONE + 16 + (tid - 320) * 4) = 0u;
  }
  auto wo_dma = [&](int i) { const int gi = w + 8 * i; if (gi < 25) dma<4>(rs, l16, OFF_WO + g * 102400 + gi * 4096, L_WO + gi * 4096); };
#pragma unroll
  for (int tt = 0; tt < 5; ++tt)
#pragma unroll
    for (int c = 0; c < 2; ++c)
      if (c == 0 || w < 7)
        *(LAS v2u*)(L + L_A + (tt * 16 + t16) * QS + (2 * w + c) * 32 + g4 * 8) =
            v2u{pack2(acc1[tt][c].x, acc1[tt][c].y), pack2(acc1[tt][c].z, acc1[tt][c].w)};
  wo_dma(0);
  bar();
  PHASE();
  {
    const int h = w & 3, par = w >> 2;
    const float cs = 0.22360679774997896f * 1.4426950408889634f;
    const int rowb = L_A + (2 * t16 + par) * QS + 40 * h;
    const int vrow = L_A + (2 * (4 * g4 + (t16 >> 2)) + par) * QS + 320 + 40 * h;
    v4u qf[2], kf[2][2], vf0[2], vf1[2];
#pragma unroll
    for (int qt = 0; qt < 2; ++qt) {
      const int qo = rowb + (16 + 32 * qt) * QS + (g4 < 3 ? 16 * g4 : 0);
      const v2u qlo = lds64(L, qo), qhi = lds64(L, qo + 8);
      qf[qt] = v4u{g4 == 3 ? 0u : qlo.x, g4 == 3 ? 0u : qlo.y, g4 >= 2 ? 0u : qhi.x, g4 >= 2 ? 0u : qhi.y};
#pragma unroll
      for (int kt = 0; kt < 2; ++kt) {
        const int ko = rowb + 160 + 16 * g4 + 32 * (qt + kt) * QS;
        const v2u klo = lds64(L, ko), khi = lds64(L, ko + 8);
        kf[qt][kt] = v4u{klo.x, klo.y, khi.x, khi.y};
      }
      const int va = vrow + 32 * qt * QS, vb = va + 32 * QS;
      const v2u a0 = ldstr(L, va + 8 * (lane & 3)), b0 = ldstr(L, vb + 8 * (lane & 3)), a1 = ldstr(L, va + 32), b1x = ldstr(L, vb + 32);
      vf0[qt] = v4u{a0.x, a0.y, b0.x, b0.y}; vf1[qt] = v4u{a1.x, a1.y, b1x.x, b1x.y};
    }
    f32x4 bias[2];
#pragma unroll
    for (int kt = 0; kt < 2; ++kt)
#pragma unroll
      for (int r = 0; r < 4; ++r) bias[kt][r] = (unsigned)(16 * kt + 4 * g4 + r - t16 - 1) < 8u ? 0.f : -1.0e30f;
    f32x4 bias0 = bias[0];
    if (p == 0 && g4 < 2) bias0 = f32x4{-1.0e30f, -1.0e30f, -1.0e30f, -1.0e30f};
    f32x4 sc[2][2];
    sc[0][0] = mfma(kf[0][0], qf[0], bias0); sc[0][1] = mfma(kf[0][1], qf[0], bias[1]);
    sc[1][0] = mfma(kf[1][0], qf[1], bias[0]); sc[1][1] = mfma(kf[1][1], qf[1], bias[1]);
    wo_dma(1);
    asm volatile("s_nop 7" : "+v"(sc[0][0]), "+v"(sc[0][1]), "+v"(sc[1][0]), "+v"(sc[1][1]));
    float mx[2], sum[2];
#pragma unroll
    for (int qt = 0; qt < 2; ++qt)
      mx[qt] = max3(max3(sc[qt][0].x, sc[qt][0].y, sc[qt][0].z), max3(sc[qt][0].w, sc[qt][1].x, sc[qt][1].y), max3(sc[qt][1].z, sc[qt][1].w, sc[qt][1].w));
#pragma unroll
    for (int qt = 0; qt < 2; ++qt) mx[qt] = x16_max(mx[qt]);
#pragma unroll
    for (int qt = 0; qt < 2; ++qt) mx[qt] = x32_max(mx[qt]) * cs;
#pragma unroll
    for (int qt = 0; qt < 2; ++qt) {
#pragma unroll
      for (int kt = 0; kt < 2; ++kt)
#pragma unroll
        for (int r = 0; r < 4; ++r) sc[qt][kt][r] = __builtin_amdgcn_exp2f(sc[qt][kt][r] * cs - mx[qt]);
      const f32x4 t4 = sc[qt][0] + sc[qt][1];
      sum[qt] = (t4.x + t4.y) + (t4.z + t4.w);
    }
    wo_dma(2);
#pragma unroll
    for (int qt = 0; qt < 2; ++qt) sum[qt] = x16_sum(sum[qt]);
#pragma unroll
    for (int qt = 0; qt < 2; ++qt) sum[qt] = __builtin_amdgcn_rcpf(x32_sum(sum[qt]));
    const int obase = L_OB + (par * 32 + t16) * OS + 40 * h;
#pragma unroll
    for (int qt = 0; qt < 2; ++qt) {
      const f32x2 iv = {sum[qt], sum[qt]};
      const v4u pf4 = {pk(sc[qt][0].xy * iv), pk(sc[qt][0].zw * iv), pk(sc[qt][1].xy * iv), pk(sc[qt][1].zw * iv)};
      const f32x4 o0 = mfma(vf0[qt], pf4, f32x4{0.f, 0.f, 0.f, 0.f});
      const f32x4 o1 = mfma(vf1[qt], pf4, f32x4{0.f, 0.f, 0.f, 0.f});
      *(LAS v2u*)(L + obase + 16 * qt * OS + 8 * g4) = v2u{pk(o0.xy), pk(o0.zw)};
      if (g4 == 0) *(LAS v2u*)(L + obase + 16 * qt * OS + 32) = v2u{pk(o1.xy), pk(o1.zw)};
    }
  }
  wo_dma(3);
  bar();
  PHASE();
  {
    const int tok = 2 * (lane & 31) + (lane >> 5);
#pragma unroll
    for (int i = 0; i < 2; ++i) {
      const int u = w + 8 * i;
      if (u < 10) {
        const int ch = 10 * g + u;
        const v4u v = lds128(L, L_OB + lane * OS + 16 * u);
        stw(rs, v, (tok >> 4) * 20480 + (tok & 15) * 16, OFF_O + p * 81920 + (ch >> 2) * 1024 + (ch & 3) * 256);
      }
    }
  }
  drain();
  bar();
  if (tid == 0) publish(ws, OFF_F1 + (p * 8 + g) * 128, T);
  PHASE();

  const int s4 = w & 3, kh = w >> 2;
  const int tokg = 64 * p + 16 * s4 + t16;
  const int fo = kh == 0 ? 8 * g4 : 32 + 8 * g4;
  const v4u xa = *(const GAS v4u*)(x + (u32)(tokg * 1280 + (80 * g + fo) * 2));
  const v2u xb = *(const GAS v2u*)(x + (u32)(tokg * 1280 + (80 * g + 64 + 4 * g4) * 2));
  v4u pv[4]; v2u b2r = {0, 0};
  if (tid < 40) {
    const u32 vo = (tid >= 20 ? 3200 * 4 + (tid - 20) * 16 : 1280 * 4 + tid * 16) + g * 320;
#pragma unroll
    for (int k = 0; k < 4; ++k) pv[k] = ldw(rs, vo, OFF_MODP + k * 15360);
  }
  if (tid >= 64 && tid < 84) b2r = *(const GAS v2u*)(b2 + (80 * g + 4 * (tid - 64)) * 2);
  if (tid >= 128 && tid < 148) {
    const v2u cp = lds64(L, L_CPK + (80 * g + 4 * (tid - 128)) * 2);
    *(LAS f32x4*)(L + L_CF + (tid - 128) * 16) = f32x4{blo(cp.x), bhi(cp.x), blo(cp.y), bhi(cp.y)};
  }
  if (w == 6) {
    if (b < 108) {
      poll256(ws, OFF_F0B, lane, T);
      v4u uw[20];
#pragma unroll
      for (int j = 0; j < 20; ++j) { uw[j] = ldw(rs, l16 + (j & 1) * 2048, OFF_W1 + (b >> 1) * 40960 + (b & 1) * 1024 + (j >> 1) * 4096); SCHED(); }
      int ua = t16 < 2 ? (t16 == 0 ? L_CPK : L_SPK) + g4 * 16 : L_ONE + (t16 == 2 ? 0 : 16);
      const int ui = t16 < 2 ? 64 : 0;
      f32x4 ud = {0.f, 0.f, 0.f, 0.f};
#pragma unroll
      for (int j = 0; j < 20; ++j) { ud = mfma(uw[j], lds128(L, ua), ud); ua += ui; }
      if (t16 < 3) stw(rs, __builtin_bit_cast(v4u, ud), (((b >> 1) * 3 + t16) * 32 + 8 * g4 + 4 * (b & 1)) * 4, OFF_UVZ);
    }
  } else if (w == 7) {
    poll256(ws, OFF_F0B, lane, T);
    poll(ws, OFF_F1 + p * 8 * 128, 8, lane, T);
  }
  if (tid < 40) {
    f32x4 s = {0.f, 0.f, 0.f, 0.f};
#pragma unroll
    for (int k = 0; k < 4; ++k) s += __builtin_bit_cast(f32x4, pv[k]);
    *(LAS f32x4*)(L + L_SM + tid * 16) = s;
  }
  if (tid >= 64 && tid < 84) *(LAS f32x4*)(L + L_SM + 640 + (tid - 64) * 16) = f32x4{blo(b2r.x), bhi(b2r.x), blo(b2r.y), bhi(b2r.y)};
  bar();
  PHASE();
  v4u oa[10];
#pragma unroll
  for (int i = 0; i < 4; ++i) { oa[i] = ldw(rs, l16 + (i & 3) * 1024, OFF_O + (p * 4 + s4) * 20480 + kh * 10240 + (i >> 2) * 4096); SCHED(); }
  SCHED();
  f32x4 acc2[5];
#pragma unroll
  for (int i = 0; i < 5; ++i) acc2[i] = f32x4{0.f, 0.f, 0.f, 0.f};
  {
    v4u wf[2][5];
    const int wb2 = L_WO + kh * 51200 + l16;
#pragma unroll
    for (int ct = 0; ct < 5; ++ct) wf[0][ct] = lds128(L, wb2 + ct * 1024);
    SCHED();
#pragma unroll
    for (int i = 0; i < 10; ++i) {
#pragma unroll
      for (int ct = 0; ct < 5; ++ct) {
        mfma_acc(acc2[ct], wf[i & 1][ct], oa[i]);
        if (i + 1 < 10) wf[(i + 1) & 1][ct] = lds128(L, wb2 + ((i + 1) * 5 + ct) * 1024);
        if (ct == 1 && i + 4 < 10) oa[i + 4] = ldw(rs, l16 + ((i + 4) & 3) * 1024, OFF_O + (p * 4 + s4) * 20480 + kh * 10240 + ((i + 4) >> 2) * 4096);
        SCHED();
      }
    }
  }
  if (kh == 0) {
    *(LAS f32x4*)(L + L_RED + (s4 * 5 + 2) * 1024 + l16) = acc2[2];
    *(LAS f32x4*)(L + L_RED + (s4 * 5 + 3) * 1024 + l16) = acc2[3];
  } else {
    *(LAS f32x4*)(L + L_RED + (s4 * 5 + 0) * 1024 + l16) = acc2[0];
    *(LAS f32x4*)(L + L_RED + (s4 * 5 + 1) * 1024 + l16) = acc2[1];
    *(LAS f32x4*)(L + L_RED + (s4 * 5 + 4) * 1024 + l16) = acc2[4];
  }
  bar();
  PHASE();
  v4u x1a; v2u x1b = {0, 0};
  {
    const int c0 = kh == 0 ? 0 : 2;
    const f32x4 u0 = (kh == 0 ? acc2[0] : acc2[2]) + ldsf4(L, L_RED + (s4 * 5 + c0) * 1024 + l16);
    const f32x4 u1 = (kh == 0 ? acc2[1] : acc2[3]) + ldsf4(L, L_RED + (s4 * 5 + c0 + 1) * 1024 + l16);
    const f32x4 g0 = ldsf4(L, L_SM + fo * 4), g1 = ldsf4(L, L_SM + fo * 4 + 16);
    x1a.x = pk(fma2(g0.xy, u0.xy, unpk(xa.x)));
    x1a.y = pk(fma2(g0.zw, u0.zw, unpk(xa.y)));
    x1a.z = pk(fma2(g1.xy, u1.xy, unpk(xa.z)));
    x1a.w = pk(fma2(g1.zw, u1.zw, unpk(xa.w)));
    const u32 xco = OFF_XC + (p * 4 + s4) * 20480 + t16 * 16;
    const f32x4 cf0 = ldsf4(L, L_CF + fo * 4), cf1 = ldsf4(L, L_CF + fo * 4 + 16);
    const v4u cpk = lds128(L, L_CPK + (80 * g + fo) * 2), spk = lds128(L, L_SPK + (80 * g + fo) * 2);
    const v4u xc = {pk(unpk(x1a.x) * cf0.xy), pk(unpk(x1a.y) * cf0.zw), pk(unpk(x1a.z) * cf1.xy), pk(unpk(x1a.w) * cf1.zw)};
    { const int ch = 10 * g + (fo >> 3); stw(rs, xc, (ch >> 2) * 1024 + (ch & 3) * 256, xco); }
    const u32 one2 = 0x3f803f80u;
    float S1 = 0.f, S2 = 0.f, T1 = 0.f, T2 = 0.f, T3 = 0.f, T4 = 0.f;
#pragma unroll
    for (int e = 0; e < 4; ++e) {
      S1 = dot2(x1a[e], one2, S1); S2 = dot2(x1a[e], x1a[e], S2); T1 = dot2(xc[e], one2, T1);
      T2 = dot2(xc[e], xc[e], T2); T3 = dot2(xc[e], cpk[e], T3); T4 = dot2(xc[e], spk[e], T4);
    }
    if (kh == 0) {
      const f32x4 u2 = acc2[4] + ldsf4(L, L_RED + (s4 * 5 + 4) * 1024 + l16);
      const f32x4 g2 = ldsf4(L, L_SM + (64 + 4 * g4) * 4);
      x1b.x = pk(fma2(g2.xy, u2.xy, unpk(xb.x)));
      x1b.y = pk(fma2(g2.zw, u2.zw, unpk(xb.y)));
      const f32x4 cf2 = ldsf4(L, L_CF + (64 + 4 * g4) * 4);
      const v2u cp2 = lds64(L, L_CPK + (80 * g + 64 + 4 * g4) * 2), sp2 = lds64(L, L_SPK + (80 * g + 64 + 4 * g4) * 2);
      const v2u xc2 = {pk(unpk(x1b.x) * cf2.xy), pk(unpk(x1b.y) * cf2.zw)};
      const int ch = 10 * g + 8 + (g4 >> 1);
      __builtin_amdgcn_raw_buffer_store_b64(xc2, rs, xco + (ch >> 2) * 1024 + (ch & 3) * 256 + (g4 & 1) * 8, 0, SC1);
#pragma unroll
      for (int e = 0; e < 2; ++e) {
        S1 = dot2(x1b[e], one2, S1); S2 = dot2(x1b[e], x1b[e], S2); T1 = dot2(xc2[e], one2, T1);
        T2 = dot2(xc2[e], xc2[e], T2); T3 = dot2(xc2[e], cp2[e], T3); T4 = dot2(xc2[e], sp2[e], T4);
      }
    }
    const float m0 = x32_sum(swap16_add(S1, S2)), m1 = x32_sum(swap16_add(T1, T2)), m2 = x32_sum(swap16_add(T3, T4));
    if (g4 < 2) stw(rs, __builtin_bit_cast(v4u, f32x4{m0, m1, m2, 0.f}), ((2 * g + kh) * 2 + g4) * 1024 + (16 * s4 + t16) * 16, OFF_MOM + p * 32768);
  }
  drain();
  bar();
  if (tid == 0) publish(ws, OFF_F2 + (p * 8 + g) * 128, T);
  if (tid == 64 && b < 108) publish(ws, OFF_FU + b * 128, T);
  PHASE();

  const int j0 = g < 7 ? 7 * g : 48, nk = g < 6 ? 7 : 6;
  const bool act3 = w < nk;
  const int jb = j0 + (act3 ? w : 0);
  constexpr int R3 = 6;
  v4u w3[R3][2];
  const u32 w1o = OFF_W1 + jb * 40960;
  v4u b1v = {0, 0, 0, 0};
  if (act3) {
#pragma unroll
    for (int j = 0; j < R3; ++j) {
      w3[j][0] = ldw(rs, l16 + (j & 1) * 2048, w1o + (j >> 1) * 4096); w3[j][1] = ldw(rs, l16 + (j & 1) * 2048 + 1024, w1o + (j >> 1) * 4096);
      SCHED();
      if (j == 2) VMCNT(4);
    }
    b1v = *(const GAS v4u*)(b1 + (32 * jb + 8 * g4) * 2);
  }
  SCHED();
  if (w == 7) poll(ws, OFF_F2 + p * 8 * 128, 8, lane, T);
  bar();
  PHASE();
#pragma unroll
  for (int i = 0; i < 3; ++i) {
    const int gi = w + 8 * i;
    if (gi < 20) dma<4>(rs, l16, OFF_XC + p * 81920 + gi * 4096, L_A + gi * 4096);
  }
  VMCNT(0);
  bar();
  PHASE();
  const u32 w2g = OFF_W2 + g * 54 * 5120;
  f32x4 acc3[4][2];
#pragma unroll
  for (int i = 0; i < 4; ++i) { acc3[i][0] = f32x4{0.f, 0.f, 0.f, 0.f}; acc3[i][1] = f32x4{0.f, 0.f, 0.f, 0.f}; }
  if (act3) {
    const int rdA = L_A + l16;
    auto rd = [&](int tt, int j) { return lds128(L, rdA + tt * 20480 + j * 1024); };
    v4u af[2][4];
#pragma unroll
    for (int tt = 0; tt < 4; ++tt) af[0][tt] = rd(tt, 0);
    SCHED();
#pragma unroll
    for (int j = 0; j < 20; ++j) {
      const int cu = j & 1, nx = cu ^ 1;
      const v4u wa = w3[j % R3][0], wb = w3[j % R3][1];
#pragma unroll
      for (int tt = 0; tt < 4; ++tt) {
        mfma_acc(acc3[tt][0], wa, af[cu][tt]);
        if (j + 1 < 20) af[nx][tt] = rd(tt, j + 1);
        SCHED();
        mfma_acc(acc3[tt][1], wb, af[cu][tt]);
        if (j + R3 < 20 && tt == 0) w3[j % R3][0] = ldw(rs, l16 + ((j + R3) & 1) * 2048, w1o + ((j + R3) >> 1) * 4096);
        if (j + R3 < 20 && tt == 2) w3[j % R3][1] = ldw(rs, l16 + ((j + R3) & 1) * 2048 + 1024, w1o + ((j + R3) >> 1) * 4096);
        SCHED();
      }
    }
  } else if (w == 7) {
    v4u uv[3], mo[32];
#pragma unroll
    for (int i = 0; i < 32; ++i) { mo[i] = ldw(rs, l16 + (i & 3) * 1024, OFF_MOM + p * 32768 + (i >> 2) * 4096); SCHED(); }
    poll(ws, OFF_FU + 2 * j0 * 128, 2 * nk, lane, T);
#pragma unroll
    for (int i = 0; i < 3; ++i) { uv[i] = ldw(rs, (i * 64 + lane < 24 * nk ? i * 64 + lane : 0) * 16, OFF_UVZ + j0 * 384); SCHED(); }
    float q5[5] = {0.f, 0.f, 0.f, 0.f, 0.f};
    {
      const u32 one2 = 0x3f803f80u;
      const int lo = (lane < 40 ? lane : 0) * 32;
#pragma unroll
      for (int hh = 0; hh < 2; ++hh) {
        const v4u cp = lds128(L, L_CPK + lo + hh * 16), sp = lds128(L, L_SPK + lo + hh * 16);
#pragma unroll
        for (int e = 0; e < 4; ++e) {
          q5[0] = dot2(cp[e], one2, q5[0]); q5[1] = dot2(cp[e], cp[e], q5[1]); q5[2] = dot2(cp[e], sp[e], q5[2]);
          q5[3] = dot2(sp[e], one2, q5[3]); q5[4] = dot2(sp[e], sp[e], q5[4]);
        }
      }
#pragma unroll
      for (int k = 0; k < 5; ++k) q5[k] = x32_sum(x16_sum(row16_sum(lane < 40 ? q5[k] : 0.f)));
    }
    f32x4 ma = {0.f, 0.f, 0.f, 0.f}, mb = {0.f, 0.f, 0.f, 0.f};
#pragma unroll
    for (int i = 0; i < 32; i += 2) { ma += __builtin_bit_cast(f32x4, mo[i]); mb += __builtin_bit_cast(f32x4, mo[i + 1]); }
    const float S1 = ma.x, T1 = ma.y, T3 = ma.z, S2 = mb.x, T2 = mb.y, T4 = mb.z;
    const float iD = 1.f / 640.f;
    const float mean = S1 * iD;
    const float r1 = __builtin_amdgcn_rsqf(fmaxf(S2 * iD - mean * mean, 0.f) + 1e-5f);
    const float H1 = r1 * (T1 - mean * q5[0]) + q5[3];
    const float H2 = r1 * r1 * (T2 - 2.f * mean * T3 + mean * mean * q5[1]) + 2.f * r1 * (T4 - mean * q5[2]) + q5[4];
    const float m2 = H1 * iD;
    const float r2 = __builtin_amdgcn_rsqf(fmaxf(H2 * iD - m2 * m2, 0.f) + 1e-5f);
    const float al = r1 * r2;
    *(LAS f32x4*)(L + L_ROWC + lane * 16) = f32x4{al, -mean * al, r2, -m2 * r2};
#pragma unroll
    for (int i = 0; i < 3; ++i) if (i * 64 + lane < 168) *(LAS v4u*)(L + L_UVZ + (i * 64 + lane) * 16) = uv[i];
  }
  bar();
  PHASE();
  if (act3) {
    const f32x2 bb[4] = {unpk(b1v.x), unpk(b1v.y), unpk(b1v.z), unpk(b1v.w)};
    const int uo = L_UVZ + w * 384 + g4 * 32;
    const f32x4 u0 = ldsf4(L, uo), u1 = ldsf4(L, uo + 16), v0 = ldsf4(L, uo + 128), v1 = ldsf4(L, uo + 144), z0 = ldsf4(L, uo + 256), z1 = ldsf4(L, uo + 272);
    const f32x2 uu[4] = {u0.xy, u0.zw, u1.xy, u1.zw}, vv[4] = {v0.xy, v0.zw, v1.xy, v1.zw}, zz[4] = {z0.xy, z0.zw, z1.xy, z1.zw};
    const float k1c = -2.f * 0.7978845608028654f * 1.4426950408889634f, k3c = k1c * 0.044715f;
#pragma unroll
    for (int tt = 0; tt < 4; ++tt) {
      const f32x4 rc = ldsf4(L, L_ROWC + (16 * tt + t16) * 16);
      const f32x2 al = {rc.x, rc.x}, be = {rc.y, rc.y}, r2 = {rc.z, rc.z}, ga2 = {rc.w, rc.w};
      v4u o;
#pragma unroll
      for (int e = 0; e < 4; ++e) {
        const f32x2 a2 = (e & 1) ? acc3[tt][e >> 1].zw : acc3[tt][e >> 1].xy;
        const f32x2 v = fma2(al, a2, fma2(be, uu[e], fma2(r2, vv[e], fma2(ga2, zz[e], bb[e]))));
        const f32x2 z = v * fma2(v * v, f32x2{k3c, k3c}, f32x2{k1c, k1c});
        const f32x2 d = f32x2{__builtin_amdgcn_exp2f(z.x), __builtin_amdgcn_exp2f(z.y)} + 1.f;
        o[e] = pk(v * f32x2{__builtin_amdgcn_rcpf(d.x), __builtin_amdgcn_rcpf(d.y)});
      }
      stw(rs, o, l16, OFF_HH + ((p * 4 + tt) * 54 + jb) * 1024);
    }
  } else if (w == 7) {
#pragma unroll 1
    for (int i = 0; i < 18; ++i) dma<4>(rs, l16, w2g + 4 * i * 1024, i * 4096);
    dma<3>(rs, l16, w2g + 72 * 1024, 72 * 1024);
    dma<4>(rs, l16, w2g + 27 * 5 * 1024, 76800);
    dma<1>(rs, l16, w2g + (27 * 5 + 4) * 1024, 76800 + 4096);
  }
  if (act3) drain();
  bar();
  if (tid == 0) publish(ws, OFF_F3 + (p * 8 + g) * 128, T);
  PHASE();

  const u32 w2o = w2g + 27 * kh * 5120;
  const u32 ring = kh * 76800;
  if (w < 7) {
#pragma unroll
    for (int i = 0; i < 3; ++i) {
      const int gi = w + 7 * i;
      if (gi < 17) dma<4>(rs, l16, w2g + (27 * 5 + 5 + 4 * gi) * 1024, 81920 + gi * 4096);
      else if (gi == 17) dma<2>(rs, l16, w2g + (27 * 5 + 5 + 68) * 1024, 81920 + 68 * 1024);
      if (i == 1) VMCNT(4);
    }
    VMCNT(0);
  } else {
    poll(ws, OFF_F3 + p * 8 * 128, 8, lane, T);
  }
  bar();
  PHASE();
  constexpr int RA4 = 18;
  v4u ha[27];
  const u32 hho = OFF_HH + ((p * 4 + s4) * 54 + 27 * kh) * 1024;
#pragma unroll
  for (int i = 0; i < RA4; ++i) {
    if (i > 0 && i % 3 == 0) asm volatile("s_barrier" ::: "memory");
    ha[i] = ldw(rs, l16 + (i & 3) * 1024, hho + (i >> 2) * 4096); SCHED();
  }
  SCHED();
  f32x4 acc4[5];
#pragma unroll
  for (int i = 0; i < 5; ++i) acc4[i] = f32x4{0.f, 0.f, 0.f, 0.f};
  {
    v4u wf[2][5];
    const int rb0 = ring + l16, rb1 = ring + 61440 + l16;
#pragma unroll
    for (int ct = 0; ct < 5; ++ct) wf[0][ct] = lds128(L, rb0 + ct * 1024);
    SCHED();
#pragma unroll
    for (int i = 0; i < 27; ++i) {
      const int n = i / 3;
#pragma unroll
      for (int ct = 0; ct < 5; ++ct) {
        mfma_acc(acc4[ct], wf[i & 1][ct], ha[i]);
        if (i + 1 < 27) wf[(i + 1) & 1][ct] = lds128(L, (((i + 1) % 15) < 12 ? rb0 : rb1) + (((i + 1) % 15) % 12 * 5 + ct) * 1024);
        if (ct == 1 && i + RA4 < 27) ha[i + RA4] = ldw(rs, l16 + ((i + RA4) & 3) * 1024, hho + ((i + RA4) >> 2) * 4096);
        SCHED();
      }
      if (i % 3 == 2) {
        if (n == 3) VMCNT(12);
        if (n == 4) VMCNT(9);
        if (n == 5) VMCNT(3);
        if (n == 6) VMCNT(0);
        bar();
        if (n < 4) {
          if (s4 < 3) dma<4>(rs, l16, w2o + (75 + 15 * n + 4 * s4) * 1024, ring + (15 * n + 4 * s4) * 1024);
          else dma<3>(rs, l16, w2o + (75 + 15 * n + 12) * 1024, ring + (15 * n + 12) * 1024);
        }
      }
    }
  }
  PHASE();
  if (kh == 0) {
    *(LAS f32x4*)(L + L_RED4 + (s4 * 5 + 2) * 1024 + l16) = acc4[2];
    *(LAS f32x4*)(L + L_RED4 + (s4 * 5 + 3) * 1024 + l16) = acc4[3];
  } else {
    *(LAS f32x4*)(L + L_RED4 + (s4 * 5 + 0) * 1024 + l16) = acc4[0];
    *(LAS f32x4*)(L + L_RED4 + (s4 * 5 + 1) * 1024 + l16) = acc4[1];
    *(LAS f32x4*)(L + L_RED4 + (s4 * 5 + 4) * 1024 + l16) = acc4[4];
  }
  bar();
  {
    const int c0 = kh == 0 ? 0 : 2;
    const f32x4 u0 = (kh == 0 ? acc4[0] : acc4[2]) + ldsf4(L, L_RED4 + (s4 * 5 + c0) * 1024 + l16);
    const f32x4 u1 = (kh == 0 ? acc4[1] : acc4[3]) + ldsf4(L, L_RED4 + (s4 * 5 + c0 + 1) * 1024 + l16);
    const f32x4 g0 = ldsf4(L, L_SM + 320 + fo * 4), g1 = ldsf4(L, L_SM + 320 + fo * 4 + 16);
    const f32x4 e0 = ldsf4(L, L_SM + 640 + fo * 4), e1 = ldsf4(L, L_SM + 640 + fo * 4 + 16);
    v4u o;
    o.x = pk(fma2(g0.xy, u0.xy + e0.xy, unpk(x1a.x)));
    o.y = pk(fma2(g0.zw, u0.zw + e0.zw, unpk(x1a.y)));
    o.z = pk(fma2(g1.xy, u1.xy + e1.xy, unpk(x1a.z)));
    o.w = pk(fma2(g1.zw, u1.zw + e1.zw, unpk(x1a.w)));
    *(GAS v4u*)(out + (u32)(tokg * 1280 + (80 * g + fo) * 2)) = o;
    if (kh == 0) {
      const f32x4 u2 = acc4[4] + ldsf4(L, L_RED4 + (s4 * 5 + 4) * 1024 + l16);
      const f32x4 g2 = ldsf4(L, L_SM + 320 + (64 + 4 * g4) * 4), e2 = ldsf4(L, L_SM + 640 + (64 + 4 * g4) * 4);
      const v2u o2 = {pk(fma2(g2.xy, u2.xy + e2.xy, unpk(x1b.x))), pk(fma2(g2.zw, u2.zw + e2.zw, unpk(x1b.y)))};
      *(GAS v2u*)(out + (u32)(tokg * 1280 + (80 * g + 64 + 4 * g4) * 2)) = o2;
    }
  }
  PHASE();
  asm volatile("fused_pf_end:" ::: "memory");
}

extern "C" void kernel_launch(void* const* d_in, const int* in_sizes, int n_in, void* d_out, int out_size, void* d_ws, size_t ws_size,
                              hipStream_t stream) {
  (void)in_sizes; (void)n_in; (void)out_size; (void)ws_size;
  (void)hipFuncSetAttribute((const void*)fused_block, hipFuncAttributeMaxDynamicSharedMemorySize, LDS_BYTES);
  fused_block<<<NWG, NTHR, LDS_BYTES, stream>>>((const u16*)d_in[0], (const u16*)d_in[1], (const u16*)d_in[2], (const u16*)d_in[4],
                                                (const u16*)d_in[5], (const u16*)d_in[6], (const u16*)d_in[7], (const u16*)d_in[8],
                                                (const u16*)d_in[9], (u16*)d_out, (unsigned char*)d_ws);
}
```

```cpp
#include <hip/hip_runtime.h>

typedef unsigned short u16;
typedef unsigned int u32;
typedef unsigned long long u64;
typedef u32 v4u __attribute__((ext_vector_type(4)));
typedef u32 v2u __attribute__((ext_vector_type(2)));
typedef float f32x4 __attribute__((ext_vector_type(4)));
typedef float f32x2 __attribute__((ext_vector_type(2)));
typedef short s16x8 __attribute__((ext_vector_type(8)));
typedef short s16x4 __attribute__((ext_vector_type(4)));
typedef __bf16 bf16x2 __attribute__((ext_vector_type(2)));
#define GAS __attribute__((address_space(1)))
#define LAS __attribute__((address_space(3)))
#define DI __device__ __forceinline__
typedef __amdgpu_buffer_rsrc_t rsrc_t;


constexpr int NWG = 256, NTHR = 512;
constexpr u32 OFF_E = 0, OFF_F0 = 0x1000, OFF_F1 = 0xA000, OFF_F2 = 0x12000, OFF_F3 = 0x1A000;
constexpr u32 OFF_MODP = 0x30000, OFF_WQ = 0x40000, OFF_WO = 0x300000, OFF_W1 = 0x400000, OFF_W2 = 0x700000;
constexpr u32 OFF_O = 0xA00000, OFF_XC = 0xD00000, OFF_HH = 0x1000000, OFF_MOM = 0x16C0000, OFF_UVZ = 0x17C0000, OFF_STAT = 0x17C8000, WS_USED = 0x1800000;
constexpr u32 OFF_FU = 0x22000, OFF_F0B = 0x26000;
constexpr int L_A = 0, L_OB = 39424, L_WO = 51200, L_JOB = 102400, L_GV = 143360, L_CNT = 145408, L_RED = 0, L_RED4 = 102400, L_SM = 153600, L_VEC = 154624, LDS_BYTES = 163840;
constexpr int L_SPK = 154624, L_CPK = 155904, L_ONE = 157184, L_CF = 157312, L_ROWC = 157696, L_UVZ = 158720, L_STAT = 161280;
constexpr int QS = 488;
constexpr int OS = 176;
constexpr int SPIN_CAP = 1 << 16;
constexpr int SC1 = 16;

DI f32x4 mfma(v4u a, v4u b, f32x4 c) {
  return __builtin_amdgcn_mfma_f32_16x16x32_bf16(__builtin_bit_cast(s16x8, a), __builtin_bit_cast(s16x8, b), c, 0, 0, 0);
}
DI void mfma_acc(f32x4& c, v4u a, v4u b) { c = mfma(a, b, c); }
DI u32 pack2(float a, float b) { f32x2 v = {a, b}; return __builtin_bit_cast(u32, __builtin_convertvector(v, bf16x2)); }
DI float blo(u32 u) { return __builtin_bit_cast(float, u << 16); }
DI float bhi(u32 u) { return __builtin_bit_cast(float, u & 0xffff0000u); }
DI f32x2 unpk(u32 u) { return f32x2{blo(u), bhi(u)}; }
DI f32x2 fma2(f32x2 a, f32x2 b, f32x2 c) { return __builtin_elementwise_fma(a, b, c); }
DI u32 pk(f32x2 v) { return __builtin_bit_cast(u32, __builtin_convertvector(v, bf16x2)); }
DI float dot2(u32 a, u32 b, float c) { return __builtin_amdgcn_fdot2_f32_bf16(__builtin_bit_cast(bf16x2, a), __builtin_bit_cast(bf16x2, b), c, false); }
DI float fma1(float a, float b, float c) { float r; asm("v_fma_f32 %0, %1, %2, %3" : "=v"(r) : "v"(a), "v"(b), "v"(c)); return r; }
DI float bf2f(u16 v) { return __builtin_bit_cast(float, (u32)v << 16); }
template <int CTRL> DI float dpp_add(float v) {
  return v + __builtin_bit_cast(float, __builtin_amdgcn_update_dpp(0, __builtin_bit_cast(int, v), CTRL, 0xf, 0xf, true));
}
DI float row16_sum(float v) { v = dpp_add<0x128>(v); v = dpp_add<0x124>(v); v = dpp_add<0x122>(v); return dpp_add<0x121>(v); }
DI float max3(float a, float b, float c) { float r; asm("v_max3_f32 %0, %1, %2, %3" : "=v"(r) : "v"(a), "v"(b), "v"(c)); return r; }
DI float max2(float a, float b) { float r; asm("v_max_f32 %0, %1, %2" : "=v"(r) : "v"(a), "v"(b)); return r; }
DI float x16_sum(float v) {
  auto r = __builtin_amdgcn_permlane16_swap(__builtin_bit_cast(u32, v), __builtin_bit_cast(u32, v), false, false);
  const u32 a = r[0], c = r[1];
  return __builtin_bit_cast(float, a) + __builtin_bit_cast(float, c);
}
DI float x32_sum(float v) {
  auto r = __builtin_amdgcn_permlane32_swap(__builtin_bit_cast(u32, v), __builtin_bit_cast(u32, v), false, false);
  const u32 a = r[0], c = r[1];
  return __builtin_bit_cast(float, a) + __builtin_bit_cast(float, c);
}
DI float x16_max(float v) {
  auto r = __builtin_amdgcn_permlane16_swap(__builtin_bit_cast(u32, v), __builtin_bit_cast(u32, v), false, false);
  const u32 a = r[0], c = r[1];
  return max2(__builtin_bit_cast(float, a), __builtin_bit_cast(float, c));
}
DI float x32_max(float v) {
  auto r = __builtin_amdgcn_permlane32_swap(__builtin_bit_cast(u32, v), __builtin_bit_cast(u32, v), false, false);
  const u32 a = r[0], c = r[1];
  return max2(__builtin_bit_cast(float, a), __builtin_bit_cast(float, c));
}
DI float swap16_add(float a, float b) {
  auto r = __builtin_amdgcn_permlane16_swap(__builtin_bit_cast(u32, a), __builtin_bit_cast(u32, b), false, false);
  const u32 x = r[0], y = r[1];
  return __builtin_bit_cast(float, x) + __builtin_bit_cast(float, y);
}
DI float swap32_add(float a, float b) {
  auto r = __builtin_amdgcn_permlane32_swap(__builtin_bit_cast(u32, a), __builtin_bit_cast(u32, b), false, false);
  const u32 x = r[0], y = r[1];
  return __builtin_bit_cast(float, x) + __builtin_bit_cast(float, y);
}
template <int V> struct IntC { static constexpr int value = V; };
template <bool V> struct BoolC { static constexpr bool value = V; };
template <int N, int I = 0, class F> DI void static_for(F&& f) {
  if constexpr (I < N) { f(IntC<I>{}); static_for<N, I + 1>(f); }
}
DI void bar() { asm volatile("s_waitcnt lgkmcnt(0)\n\ts_barrier" ::: "memory"); }
DI void drain() { asm volatile("s_waitcnt vmcnt(0)" ::: "memory"); }
#define SCHED() __builtin_amdgcn_sched_barrier(0)
#define VMCNT(n) asm volatile("s_waitcnt vmcnt(" #n ")" ::: "memory")
DI v4u ldw(rsrc_t rs, u32 voff, u32 soff) { return __builtin_amdgcn_raw_buffer_load_b128(rs, voff, soff, SC1); }
DI void stw(rsrc_t rs, v4u v, u32 voff, u32 soff) { __builtin_amdgcn_raw_buffer_store_b128(v, rs, voff + soff, 0, SC1); }
DI v4u lds128(LAS unsigned char* L, int off) { return *(LAS v4u*)(L + off); }
DI f32x4 ldsf4(LAS unsigned char* L, int off) { return *(LAS f32x4*)(L + off); }
DI v2u lds64(LAS unsigned char* L, int off) { return *(LAS v2u*)(L + off); }
DI v2u ldstr(LAS unsigned char* L, int off) {
  return __builtin_bit_cast(v2u, __builtin_amdgcn_ds_read_tr16_b64_v4i16((LAS s16x4*)(L + off)));
}
template <int N> DI void dma(rsrc_t rs, u32 voff, u32 soff, u32 ldsoff) {
#define DMA_I(o) "buffer_load_dwordx4 %1, %2, %3 offen offset:" #o " sc1 lds\n\t"
#define DMA_ARGS :: "s"(ldsoff), "v"(voff), "s"(rs), "s"(soff) : "memory"
  if constexpr (N == 4) asm volatile("s_mov_b32 m0, %0\n\ts_nop 0\n\t" DMA_I(0) DMA_I(1024) DMA_I(2048) DMA_I(3072) DMA_ARGS);
  else if constexpr (N == 3) asm volatile("s_mov_b32 m0, %0\n\ts_nop 0\n\t" DMA_I(0) DMA_I(1024) DMA_I(2048) DMA_ARGS);
  else if constexpr (N == 2) asm volatile("s_mov_b32 m0, %0\n\ts_nop 0\n\t" DMA_I(0) DMA_I(1024) DMA_ARGS);
  else asm volatile("s_mov_b32 m0, %0\n\ts_nop 0\n\t" DMA_I(0) DMA_ARGS);
}
DI void poll(unsigned char* ws, u32 off, int n, int lane, u64 T) {
  const GAS u64* f = (const GAS u64*)(ws + off + (lane < n ? lane : 0) * 128);
#pragma unroll 1
  for (int spins = 0; spins < SPIN_CAP; ++spins) {
    const u64 v = __hip_atomic_load(f, __ATOMIC_RELAXED, __HIP_MEMORY_SCOPE_AGENT);
    if (__all(v == T)) break;
  }
  __builtin_amdgcn_fence(__ATOMIC_ACQUIRE, "wavefront");
}
DI void poll256(unsigned char* ws, u32 off, int lane, u64 T) {
  const GAS u64* f = (const GAS u64*)(ws + off + lane * 32);
#pragma unroll 1
  for (int spins = 0; spins < SPIN_CAP; ++spins) {
    const u64 v0 = __hip_atomic_load(f, __ATOMIC_RELAXED, __HIP_MEMORY_SCOPE_AGENT);
    const u64 v1 = __hip_atomic_load(f + 256, __ATOMIC_RELAXED, __HIP_MEMORY_SCOPE_AGENT);
    const u64 v2 = __hip_atomic_load(f + 512, __ATOMIC_RELAXED, __HIP_MEMORY_SCOPE_AGENT);
    const u64 v3 = __hip_atomic_load(f + 768, __ATOMIC_RELAXED, __HIP_MEMORY_SCOPE_AGENT);
    if (__all(v0 == T && v1 == T && v2 == T && v3 == T)) break;
  }
  __builtin_amdgcn_fence(__ATOMIC_ACQUIRE, "wavefront");
}
DI void publish(unsigned char* ws, u32 off, u64 T) {
  __hip_atomic_store((GAS u64*)(ws + off), T, __ATOMIC_RELAXED, __HIP_MEMORY_SCOPE_AGENT);
}
#define PHASE() asm volatile("" ::: "memory")

typedef const GAS unsigned char* gptr;
__global__ __launch_bounds__(NTHR, 2) void fused_block(const u16* a_x, const u16* a_wqkv, const u16* a_wo, const u16* a_w1, const u16* a_b1,
                                                       const u16* a_w2, const u16* a_b2, const u16* a_c, const u16* a_wmod, u16* a_out,
                                                       unsigned char* a_ws) {
  extern __shared__ __attribute__((aligned(16))) unsigned char smem[];
  const __attribute__((address_space(4))) u64* ka = (const __attribute__((address_space(4))) u64*)__builtin_amdgcn_kernarg_segment_ptr();
  u64 k0 = ka[0], k1 = ka[1], k2 = ka[2], k3 = ka[3], k4 = ka[4], k5 = ka[5], k6 = ka[6], k7 = ka[7], k8 = ka[8], k9 = ka[9], k10 = ka[10];
  asm volatile("" : "+s"(k0), "+s"(k1), "+s"(k2), "+s"(k3), "+s"(k4), "+s"(k5), "+s"(k6), "+s"(k7), "+s"(k8), "+s"(k9), "+s"(k10));
  const gptr x = (gptr)k0, wqkv = (gptr)k1, wo = (gptr)k2, w1 = (gptr)k3, b1 = (gptr)k4, w2 = (gptr)k5, b2 = (gptr)k6, cvec = (gptr)k7, wmod = (gptr)k8;
  GAS unsigned char* const out = (GAS unsigned char*)k9;
  unsigned char* const ws = (unsigned char*)k10;
  LAS unsigned char* L = (LAS unsigned char*)smem;
  const int tid = threadIdx.x, lane = tid & 63, b = blockIdx.x;
  const int w = __builtin_amdgcn_readfirstlane(tid >> 6);
  const int xcd = b & 7, jj = b >> 3;
  const int p = (xcd >> 1) * 8 + (jj >> 2), g = (xcd & 1) * 4 + (jj & 3);
  const rsrc_t rs = __builtin_amdgcn_make_buffer_rsrc(ws, 0, WS_USED, 0x00020000);
  const int g4 = lane >> 4, t16 = lane & 15;
  const u32 l16 = lane * 16;
  PHASE();

  u64 Ev = 0;
  if (w == 3) Ev = __hip_atomic_load((const GAS u64*)(ws + OFF_E), __ATOMIC_RELAXED, __HIP_MEMORY_SCOPE_AGENT);
  if (tid == 0) { *(LAS u32*)(L + L_CNT) = 0u; *(LAS u32*)(L + L_CNT + 4) = 0u; }
  bar();
  const int id2 = (w - 4) * 256 + b;
  const bool hasjob = w < 4 ? w * 256 + b < 640 : id2 < 972;
  const int job = w < 4 ? (hasjob ? w * 256 + b : 0) : (id2 < 540 ? 1072 + id2 : (hasjob ? 640 + id2 - 540 : 1072));
  gptr jsrc; u32 jstride, jdst, jA, jB; int ppr; bool paired;
  if (job < 480) {
    const int gg = job / 60, rem = job % 60, part = rem / 20, j = rem % 20;
    jsrc = wqkv + (u32)(32 * j * 3840 + part * 1280 + gg * 160);
    jstride = 3840; ppr = 10; paired = false;
    jdst = OFF_WQ + (gg * 15 + part * 5) * 20480 + j * 1024; jA = 40960; jB = 20480;
  } else if (job < 640) {
    const int id = job - 480, gg = id / 20, j = id % 20;
    jsrc = wo + (u32)(32 * j * 1280 + gg * 160);
    jstride = 1280; ppr = 10; paired = true;
    jdst = OFF_WO + (gg * 20 + j) * 5120; jA = 2048; jB = 1024;
  } else if (job < 1072) {
    const int id = job - 640, gg = id / 54, j = id % 54;
    jsrc = w2 + (u32)(32 * j * 1280 + gg * 160);
    jstride = 1280; ppr = 10; paired = true;
    jdst = OFF_W2 + (gg * 54 + j) * 5120; jA = 2048; jB = 1024;
  } else {
    const int id = job - 1072, jb2 = id / 20, j = id % 20;
    jsrc = w1 + (u32)(32 * j * 3456 + jb2 * 128);
    jstride = 3456; ppr = 8; paired = true;
    jdst = OFF_W1 + (2 * jb2 * 20 + j) * 2048; jA = 40960; jB = 1024;
  }
    const int cl = b % 60, ks = b < 240 ? b / 60 : 3;
  v4u jr[5];
  auto job_loads = [&]() {
    const u32 magic = ppr == 10 ? 6554u : 8192u;
#pragma unroll
    for (int it = 0; it < 5; ++it) {
      u32 idx = it * 64 + lane;
      if (it == 4 && ppr == 8) idx = lane;
      const u32 row = (idx * magic) >> 16, pc = idx - row * ppr;
      jr[it] = *(const GAS v4u*)(jsrc + (row * jstride + pc * 16));
      SCHED();
    }
  };
  auto job_write = [&]() {
    LAS unsigned char* sc = L + L_JOB + w * 5120;
#pragma unroll
    for (int it = 0; it < 5; ++it)
      if (it < 4 || ppr == 10) *(LAS v4u*)(sc + it * 1024 + l16) = jr[it];
    __builtin_amdgcn_fence(__ATOMIC_RELEASE, "wavefront");
    __builtin_amdgcn_wave_barrier();
    __builtin_amdgcn_fence(__ATOMIC_ACQUIRE, "wavefront");
  };
  auto job_tile = [&](int ct) {
    if (ct < 4 || ppr == 10) {
      LAS unsigned char* sc = L + L_JOB + w * 5120;
      const int rowb = ppr * 16, rbase = (8 * g4 + (t16 >> 2)) * rowb, pp = lane & 3;
      const int cb = (paired && ct < 4) ? 64 * (ct >> 1) + 8 * (ct & 1) + 16 * pp : 32 * ct + 8 * pp;
      const v2u lo = ldstr(sc, rbase + cb), hi = ldstr(sc, rbase + 4 * rowb + cb);
      stw(rs, v4u{lo.x, lo.y, hi.x, hi.y}, l16, jdst + (ct >> 1) * jA + (ct & 1) * jB);
    }
  };
  auto run_job = [&]() {
    job_write();
#pragma unroll
    for (int ct = 0; ct < 5; ++ct) job_tile(ct);
  };
  v4u xr[2][5]; v2u xh[5];
  u32 xo[3];
#pragma unroll
  for (int a = 0; a < 3; ++a) {
    const int tok = 64 * p - 16 + 4 * (a < 2 ? w + 8 * a : (w & 3) + 16) + g4;
    xo[a] = (tok < 0 ? 0 : tok) * 1280 + t16 * 16 + (a == 2 ? (w >> 2) * 8 : 0);
  }
  auto x_chunk = [&](int m) {
    xr[0][m] = *(const GAS v4u*)(x + (xo[0] + m * 256)); SCHED();
    xr[1][m] = *(const GAS v4u*)(x + (xo[1] + m * 256)); SCHED();
    xh[m] = *(const GAS v2u*)(x + (xo[2] + m * 256)); SCHED();
  };
  auto lds_add = [&](int off) { __hip_atomic_fetch_add((LAS u32*)(L + off), 1u, __ATOMIC_RELAXED, __HIP_MEMORY_SCOPE_WORKGROUP); };
  auto lds_wait = [&](int off, u32 n) {
#pragma unroll 1
    for (int spins = 0; spins < SPIN_CAP * 16; ++spins) {
      if (__hip_atomic_load((LAS u32*)(L + off), __ATOMIC_RELAXED, __HIP_MEMORY_SCOPE_WORKGROUP) >= n) break;
      __builtin_amdgcn_s_sleep(1);
    }
    asm volatile("" ::: "memory");
  };
  u32 pf = 0;
  if (w < 4) {
    v4u gw[5];
#pragma unroll
    for (int i = 0; i < 5; ++i) { gw[i] = *(const GAS v4u*)(wmod + (u32)((160 * ks + 40 * w + (lane >> 3) + 8 * i) * 7680 + cl * 128 + (lane & 7) * 16)); SCHED(); }
    const u16 gc = *(const GAS u16*)(cvec + (160 * ks + 40 * w + (lane < 40 ? lane : 0)) * 2);
    SCHED();
    v4u sr[10];
    if (w == 3) {
#pragma unroll
      for (int i = 0; i < 10; ++i) { sr[i] = *(const GAS v4u*)(x + (u32)((8 * b + (lane >> 3)) * 1280 + ((lane & 7) + 8 * i) * 16)); SCHED(); }
    } else {
      job_loads();
    }
    asm volatile("s_barrier" ::: "memory");
    x_chunk(0); x_chunk(1); x_chunk(2);
    PHASE();
    const float c = bf2f(gc);
    const float sl = c * __builtin_amdgcn_rcpf(1.f + __expf(-c));
    f32x2 acc[4];
#pragma unroll
    for (int i = 0; i < 5; ++i) {
      const float sc = __builtin_bit_cast(float, __builtin_amdgcn_ds_bpermute(((lane >> 3) + 8 * i) * 4, __builtin_bit_cast(int, sl)));
      const f32x2 s2 = {sc, sc};
#pragma unroll
      for (int e = 0; e < 4; ++e) acc[e] = i == 0 ? unpk(gw[i][e]) * s2 : fma2(unpk(gw[i][e]), s2, acc[e]);
    }
    float u[8];
#pragma unroll
    for (int e = 0; e < 4; ++e) { u[2 * e] = dpp_add<0x128>(acc[e].x); u[2 * e + 1] = dpp_add<0x128>(acc[e].y); }
    float v[4];
#pragma unroll
    for (int e = 0; e < 4; ++e) v[e] = swap16_add(u[2 * e], u[2 * e + 1]);
    const float z0 = swap32_add(v[0], v[1]), z1 = swap32_add(v[2], v[3]);
    const float z = (lane & 8) ? z1 : z0;
    const int col = (lane & 7) * 8 + ((lane >> 1) & 4) + ((lane >> 4) & 2) + ((lane >> 4) & 1);
    *(LAS float*)(L + L_GV + w * 256 + col * 4) = z;
    asm volatile("s_waitcnt lgkmcnt(0)" ::: "memory");
    if (lane == 0) lds_add(L_CNT);
    PHASE();
    if (w == 3) {
      float s0 = 0.f, s1 = 0.f, q0 = 0.f, q1 = 0.f;
#pragma unroll
      for (int i = 0; i < 10; ++i) {
        s0 = dot2(sr[i].x, 0x3f803f80u, s0); q0 = dot2(sr[i].x, sr[i].x, q0); s1 = dot2(sr[i].y, 0x3f803f80u, s1); q1 = dot2(sr[i].y, sr[i].y, q1);
        s0 = dot2(sr[i].z, 0x3f803f80u, s0); q0 = dot2(sr[i].z, sr[i].z, q0); s1 = dot2(sr[i].w, 0x3f803f80u, s1); q1 = dot2(sr[i].w, sr[i].w, q1);
      }
      const float mean = dpp_add<0x141>(dpp_add<0x4e>(dpp_add<0xb1>(s0 + s1))) * (1.f / 640.f);
      const float var = fmaxf(dpp_add<0x141>(dpp_add<0x4e>(dpp_add<0xb1>(q0 + q1))) * (1.f / 640.f) - mean * mean, 0.f);
      const float rstd = __builtin_amdgcn_rsqf(var + 1e-5f);
      if ((lane & 7) == 0)
        __builtin_amdgcn_raw_buffer_store_b64(__builtin_bit_cast(v2u, f32x2{rstd, -mean * rstd}), rs, (8 * b + (lane >> 3)) * 8, OFF_STAT, SC1);
      lds_wait(L_CNT, 4);
      const float s = (*(LAS float*)(L + L_GV + lane * 4) + *(LAS float*)(L + L_GV + 256 + lane * 4)) +
                      (*(LAS float*)(L + L_GV + 512 + lane * 4) + *(LAS float*)(L + L_GV + 768 + lane * 4));
      if (b < 240) __builtin_amdgcn_raw_buffer_store_b32(__builtin_bit_cast(u32, s), rs, lane * 4, OFF_MODP + (ks * 3840 + cl * 64) * 4, SC1);
    } else if (hasjob) {
      run_job();
    }
    drain();
    if (lane == 0) lds_add(L_CNT + 4);
    if (w == 3) {
      lds_wait(L_CNT + 4, 4);
      u64 e3 = Ev;
      asm volatile("" : "+v"(e3));
      if (lane == 0) publish(ws, OFF_F0 + b * 32, e3 + 1);
    }
  } else {
    if (w == 7) {
      u64 pc; int len;
      asm volatile("fused_pf_begin:\n\ts_getpc_b64 %0\n\ts_mov_b32 %1, fused_pf_end-fused_pf_begin" : "=s"(pc), "=s"(len));
      const int o = (jj * 16 + t16) * 128;
      if (o < len) pf = *(const GAS u32*)(pc + (u32)o);
    }
    asm volatile("s_barrier" ::: "memory");
    x_chunk(0); x_chunk(1); x_chunk(2);
  }
  if (w == 3) {
    u64 e3 = Ev;
    asm volatile("" : "+v"(e3));
    poll256(ws, OFF_F0, lane, e3 + 1);
    if (b == 0 && lane == 0) publish(ws, OFF_E, e3 + 1);
    v4u pv1[5][4]; v2u st[2];
#pragma unroll
    for (int r = 0; r < 5; ++r)
#pragma unroll
      for (int k = 0; k < 4; ++k) { pv1[r][k] = ldw(rs, (r * 64 + lane) * 16, OFF_MODP + k * 15360); SCHED(); }
#pragma unroll
    for (int r = 0; r < 2; ++r) {
      int tok = 64 * p - 16 + r * 64 + (r == 0 || lane < 16 ? lane : 0);
      tok = tok < 0 ? 0 : tok;
      st[r] = __builtin_amdgcn_raw_buffer_load_b64(rs, tok * 8, OFF_STAT, SC1);
    }
    SCHED();
#pragma unroll
    for (int r = 0; r < 5; ++r) {
      const f32x4 s = (__builtin_bit_cast(f32x4, pv1[r][0]) + __builtin_bit_cast(f32x4, pv1[r][1])) + (__builtin_bit_cast(f32x4, pv1[r][2]) + __builtin_bit_cast(f32x4, pv1[r][3]));
      const int qd = r * 64 + lane, col = (qd < 160 ? qd : qd - 160) * 4;
      const int vo = L_VEC + (col >> 3) * 80 + (col & 4) * 4;
      if (qd < 160) *(LAS f32x4*)(L + vo + 32) = s;
      else *(LAS f32x4*)(L + vo) = s + 1.f;
    }
    *(LAS v2u*)(L + L_STAT + lane * 8) = st[0];
    if (lane < 16) *(LAS v2u*)(L + L_STAT + 512 + lane * 8) = st[1];
    if (lane == 0) *(LAS u64*)(L + L_CNT + 8) = e3 + 1;
  }
  PHASE();
  asm volatile("" ::"v"(pf));
  bar();
  const v2u Tv = lds64(L, L_CNT + 8);
  const u64 T = ((u64)__builtin_amdgcn_readfirstlane(Tv.y) << 32) | __builtin_amdgcn_readfirstlane(Tv.x);

  constexpr int R1 = 6;
  v4u wr[R1][2];
  const u32 wq0 = OFF_WQ + (g * 15 + 2 * w) * 20480, wq1 = OFF_WQ + (g * 15 + (w < 7 ? 2 * w + 1 : 14)) * 20480;
  auto ring1 = [&](int j) {
    wr[j][0] = ldw(rs, l16 + (j & 3) * 1024, wq0 + (j >> 2) * 4096); wr[j][1] = ldw(rs, l16 + (j & 3) * 1024, wq1 + (j >> 2) * 4096);
    SCHED();
  };
  ring1(0);
  f32x4 acc1[5][2];
#pragma unroll
  for (int i = 0; i < 5; ++i) { acc1[i][0] = f32x4{0.f, 0.f, 0.f, 0.f}; acc1[i][1] = f32x4{0.f, 0.f, 0.f, 0.f}; }
  const int rd_even = g4 * 256 + ((t16 ^ g4) * 16), rd_odd = rd_even ^ 64;
  int wad[3];
  float xrs[3], xnb[3];
#pragma unroll
  for (int a = 0; a < 3; ++a) {
    const int grp = a < 2 ? w + 8 * a : (w & 3) + 16, tt = grp >> 2, t = 4 * (grp & 3) + g4;
    wad[a] = L_A + tt * 20480 + (t16 >> 2) * 1024 + (t16 & 3) * 256 + ((t ^ (t16 & 7)) * 16) + (a == 2 ? (w >> 2) * 8 : 0);
    const v2u st = lds64(L, L_STAT + (4 * grp + g4) * 8);
    xrs[a] = __builtin_bit_cast(float, (u32)st.x); xnb[a] = __builtin_bit_cast(float, (u32)st.y);
  }
  const int vad = L_VEC + t16 * 80, vah = vad + (w >> 2) * 16;
  f32x4 cm[3], sv[3];
  auto ldmod = [&](int m) {
    cm[0] = ldsf4(L, vad + m * 1280); sv[0] = ldsf4(L, vad + m * 1280 + 32);
    cm[1] = ldsf4(L, vad + m * 1280 + 16); sv[1] = ldsf4(L, vad + m * 1280 + 48);
    cm[2] = ldsf4(L, vah + m * 1280); sv[2] = ldsf4(L, vah + m * 1280 + 32);
  };
#pragma unroll
  for (int m = 0; m < 2; ++m) {
    ldmod(m);
#pragma unroll
    for (int a = 0; a < 2; ++a) {
      const f32x2 r2 = {xrs[a], xrs[a]}, n2 = {xnb[a], xnb[a]};
      *(LAS v4u*)(L + wad[a] + m * 4096) =
          v4u{pk(fma2(fma2(unpk(xr[a][m].x), r2, n2), cm[0].xy, sv[0].xy)), pk(fma2(fma2(unpk(xr[a][m].y), r2, n2), cm[0].zw, sv[0].zw)),
              pk(fma2(fma2(unpk(xr[a][m].z), r2, n2), cm[1].xy, sv[1].xy)), pk(fma2(fma2(unpk(xr[a][m].w), r2, n2), cm[1].zw, sv[1].zw))};
      SCHED();
      ring1(1 + 3 * m + a);
    }
    const f32x2 r2 = {xrs[2], xrs[2]}, n2 = {xnb[2], xnb[2]};
    *(LAS v2u*)(L + wad[2] + m * 4096) = v2u{pk(fma2(fma2(unpk(xh[m].x), r2, n2), cm[2].xy, sv[2].xy)), pk(fma2(fma2(unpk(xh[m].y), r2, n2), cm[2].zw, sv[2].zw))};
    SCHED();
    if (m == 0) ring1(3);
  }
  bar();
  PHASE();
  v4u pvs[4];
  {
    int rdA[2] = {L_A + rd_even, L_A + rd_odd}, rdB[2] = {L_A + 81920 + rd_even, L_A + 81920 + rd_odd};
    asm volatile("" : "+v"(rdA[0]), "+v"(rdA[1]), "+v"(rdB[0]), "+v"(rdB[1]));
    auto rd = [&](int tt, int j) { return tt < 4 ? lds128(L, rdA[j & 1] + tt * 20480 + j * 1024) : lds128(L, rdB[j & 1] + j * 1024); };
    v4u af[2][5];
#pragma unroll
    for (int tt = 0; tt < 5; ++tt) af[0][tt] = rd(tt, 0);
    SCHED();
    float pu[8]; v4u po;
    auto uop = [&](auto oc, auto mc) {
      constexpr int o = decltype(oc)::value, mp = decltype(mc)::value;
      if constexpr (o < 58) {
        constexpr int a = o / 29, i = o % 29;
        constexpr int kind[29] = {0, 0, 0, 0, 1, 1, 0, 0, 1, 1, 0, 0, 2, 2, 1, 1, 2, 2, 1, 1, 3, 2, 2, 3, 2, 2, 3, 3, 4};
        constexpr int el[29] = {0, 1, 2, 3, 0, 1, 4, 5, 2, 3, 6, 7, 0, 1, 4, 5, 2, 3, 6, 7, 0, 4, 5, 1, 6, 7, 2, 3, 0};
        constexpr int e = el[i];
        if constexpr (kind[i] == 0) pu[e] = (e & 1) ? bhi(xr[a][mp][e >> 1]) : blo(xr[a][mp][e >> 1]);
        else if constexpr (kind[i] == 1) pu[e] = fma1(pu[e], xrs[a], xnb[a]);
        else if constexpr (kind[i] == 2) pu[e] = fma1(pu[e], cm[e >> 2][e & 3], sv[e >> 2][e & 3]);
        else if constexpr (kind[i] == 3) po[e] = pk(f32x2{pu[2 * e], pu[2 * e + 1]});
        else *(LAS v4u*)(L + wad[a] + mp * 4096) = po;
      } else {
        constexpr int i = o - 58;
        constexpr int kind[15] = {0, 0, 0, 0, 1, 1, 1, 1, 2, 2, 2, 2, 3, 3, 4};
        constexpr int el[15] = {0, 1, 2, 3, 0, 1, 2, 3, 0, 1, 2, 3, 0, 1, 0};
        constexpr int e = el[i];
        if constexpr (kind[i] == 0) pu[e] = (e & 1) ? bhi(xh[mp][e >> 1]) : blo(xh[mp][e >> 1]);
        else if constexpr (kind[i] == 1) pu[e] = fma1(pu[e], xrs[2], xnb[2]);
        else if constexpr (kind[i] == 2) pu[e] = fma1(pu[e], cm[2][e], sv[2][e]);
        else if constexpr (kind[i] == 3) po[e] = pk(f32x2{pu[2 * e], pu[2 * e + 1]});
        else *(LAS v2u*)(L + wad[2] + mp * 4096) = v2u{po.x, po.y};
      }
    };
    static_for<20>([&](auto jc) {
      constexpr int j = decltype(jc)::value;
      constexpr int m = j >> 2, q = j & 3, cu = j & 1, nx = cu ^ 1, mp = m + 2 < 5 ? m + 2 : 4;
      constexpr bool more = j + 1 < 20, fill = j + R1 < 20, prep = m < 3;
      if constexpr (j == 7) { if (w >= 4) job_loads(); }
      if constexpr (j == 12) { if (w >= 4 && hasjob) job_write(); }
      if constexpr (j >= 13 && j < 18) { if (w >= 4 && hasjob) job_tile(j - 13); }
      if constexpr (j == 16) {
        const u32 vo = 1920 * 4 + (tid < 320 ? tid : 0) * 16;
#pragma unroll
        for (int k = 0; k < 4; ++k) { pvs[k] = ldw(rs, vo, OFF_MODP + k * 15360); SCHED(); }
      }
      const v4u wa = wr[j % R1][0], wb = wr[j % R1][1];
      static_for<10>([&](auto sc) {
        constexpr int sl = decltype(sc)::value, tt = sl >> 1, S = q * 10 + sl;
        mfma_acc(acc1[tt][sl & 1], (sl & 1) ? wb : wa, af[cu][tt]);
        if constexpr (more && !(sl & 1)) af[nx][tt] = rd(tt, j + 1);
        if constexpr (fill && sl == 3) wr[j % R1][0] = ldw(rs, l16 + ((j + R1) & 3) * 1024, wq0 + ((j + R1) >> 2) * 4096);
        if constexpr (fill && sl == 7) wr[j % R1][1] = ldw(rs, l16 + ((j + R1) & 3) * 1024, wq1 + ((j + R1) >> 2) * 4096);
        if constexpr (prep && S == 0) ldmod(mp);
        if constexpr (m < 2 && S == 5) x_chunk(m + 3);
        if constexpr (prep && S >= 2) {
          constexpr int o0 = (S - 2) * 73 / 38, o1 = (S - 1) * 73 / 38;
          static_for<o1 - o0>([&](auto oc) { uop(IntC<o0 + decltype(oc)::value>{}, IntC<mp>{}); });
        }
        SCHED();
      });
      if constexpr (m < 3 && q == 3) bar();
    });
  }
  if (w >= 4) drain();
  bar();
  PHASE();
  if (tid == 256) publish(ws, OFF_F0B + b * 32, T);
  if (tid < 320) {
    f32x4 s = (__builtin_bit_cast(f32x4, pvs[0]) + __builtin_bit_cast(f32x4, pvs[1])) + (__builtin_bit_cast(f32x4, pvs[2]) + __builtin_bit_cast(f32x4, pvs[3]));
    if (tid >= 160) s += 1.f;
    *(LAS v2u*)(L + L_SPK + tid * 8) = v2u{pk(s.xy), pk(s.zw)};
  } else if (tid < 324) {
    *(LAS u32*)(L + L_ONE + (tid - 320) * 4) = 0x3f803f80u;
    *(LAS u32*)(L + L_ONE + 16 + (tid - 320) * 4) = 0u;
  }
  auto wo_dma = [&](int i) { const int gi = w + 8 * i; if (gi < 25) dma<4>(rs, l16, OFF_WO + g * 102400 + gi * 4096, L_WO + gi * 4096); };
#pragma unroll
  for (int tt = 0; tt < 5; ++tt)
#pragma unroll
    for (int c = 0; c < 2; ++c)
      if (c == 0 || w < 7)
        *(LAS v2u*)(L + L_A + (tt * 16 + t16) * QS + (2 * w + c) * 32 + g4 * 8) =
            v2u{pack2(acc1[tt][c].x, acc1[tt][c].y), pack2(acc1[tt][c].z, acc1[tt][c].w)};
  wo_dma(0);
  bar();
  PHASE();
  {
    const int h = w & 3, par = w >> 2;
    const float cs = 0.22360679774997896f * 1.4426950408889634f;
    const int rowb = L_A + (2 * t16 + par) * QS + 40 * h;
    const int vrow = L_A + (2 * (4 * g4 + (t16 >> 2)) + par) * QS + 320 + 40 * h;
    v4u qf[2], kf[2][2], vf0[2], vf1[2];
#pragma unroll
    for (int qt = 0; qt < 2; ++qt) {
      const int qo = rowb + (16 + 32 * qt) * QS + (g4 < 3 ? 16 * g4 : 0);
      const v2u qlo = lds64(L, qo), qhi = lds64(L, qo + 8);
      qf[qt] = v4u{g4 == 3 ? 0u : qlo.x, g4 == 3 ? 0u : qlo.y, g4 >= 2 ? 0u : qhi.x, g4 >= 2 ? 0u : qhi.y};
#pragma unroll
      for (int kt = 0; kt < 2; ++kt) {
        const int ko = rowb + 160 + 16 * g4 + 32 * (qt + kt) * QS;
        const v2u klo = lds64(L, ko), khi = lds64(L, ko + 8);
        kf[qt][kt] = v4u{klo.x, klo.y, khi.x, khi.y};
      }
      const int va = vrow + 32 * qt * QS, vb = va + 32 * QS;
      const v2u a0 = ldstr(L, va + 8 * (lane & 3)), b0 = ldstr(L, vb + 8 * (lane & 3)), a1 = ldstr(L, va + 32), b1x = ldstr(L, vb + 32);
      vf0[qt] = v4u{a0.x, a0.y, b0.x, b0.y}; vf1[qt] = v4u{a1.x, a1.y, b1x.x, b1x.y};
    }
    f32x4 bias[2];
#pragma unroll
    for (int kt = 0; kt < 2; ++kt)
#pragma unroll
      for (int r = 0; r < 4; ++r) bias[kt][r] = (unsigned)(16 * kt + 4 * g4 + r - t16 - 1) < 8u ? 0.f : -1.0e30f;
    f32x4 bias0 = bias[0];
    if (p == 0 && g4 < 2) bias0 = f32x4{-1.0e30f, -1.0e30f, -1.0e30f, -1.0e30f};
    f32x4 sc[2][2];
    sc[0][0] = mfma(kf[0][0], qf[0], bias0); sc[0][1] = mfma(kf[0][1], qf[0], bias[1]);
    sc[1][0] = mfma(kf[1][0], qf[1], bias[0]); sc[1][1] = mfma(kf[1][1], qf[1], bias[1]);
    wo_dma(1);
    asm volatile("s_nop 7" : "+v"(sc[0][0]), "+v"(sc[0][1]), "+v"(sc[1][0]), "+v"(sc[1][1]));
    float mx[2], sum[2];
#pragma unroll
    for (int qt = 0; qt < 2; ++qt)
      mx[qt] = max3(max3(sc[qt][0].x, sc[qt][0].y, sc[qt][0].z), max3(sc[qt][0].w, sc[qt][1].x, sc[qt][1].y), max3(sc[qt][1].z, sc[qt][1].w, sc[qt][1].w));
#pragma unroll
    for (int qt = 0; qt < 2; ++qt) mx[qt] = x16_max(mx[qt]);
#pragma unroll
    for (int qt = 0; qt < 2; ++qt) mx[qt] = x32_max(mx[qt]) * cs;
#pragma unroll
    for (int qt = 0; qt < 2; ++qt) {
#pragma unroll
      for (int kt = 0; kt < 2; ++kt)
#pragma unroll
        for (int r = 0; r < 4; ++r) sc[qt][kt][r] = __builtin_amdgcn_exp2f(sc[qt][kt][r] * cs - mx[qt]);
      const f32x4 t4 = sc[qt][0] + sc[qt][1];
      sum[qt] = (t4.x + t4.y) + (t4.z + t4.w);
    }
    wo_dma(2);
#pragma unroll
    for (int qt = 0; qt < 2; ++qt) sum[qt] = x16_sum(sum[qt]);
#pragma unroll
    for (int qt = 0; qt < 2; ++qt) sum[qt] = __builtin_amdgcn_rcpf(x32_sum(sum[qt]));
    const int obase = L_OB + (par * 32 + t16) * OS + 40 * h;
#pragma unroll
    for (int qt = 0; qt < 2; ++qt) {
      const f32x2 iv = {sum[qt], sum[qt]};
      const v4u pf4 = {pk(sc[qt][0].xy * iv), pk(sc[qt][0].zw * iv), pk(sc[qt][1].xy * iv), pk(sc[qt][1].zw * iv)};
      const f32x4 o0 = mfma(vf0[qt], pf4, f32x4{0.f, 0.f, 0.f, 0.f});
      const f32x4 o1 = mfma(vf1[qt], pf4, f32x4{0.f, 0.f, 0.f, 0.f});
      *(LAS v2u*)(L + obase + 16 * qt * OS + 8 * g4) = v2u{pk(o0.xy), pk(o0.zw)};
      if (g4 == 0) *(LAS v2u*)(L + obase + 16 * qt * OS + 32) = v2u{pk(o1.xy), pk(o1.zw)};
    }
  }
  wo_dma(3);
  bar();
  PHASE();
  {
    const int tok = 2 * (lane & 31) + (lane >> 5);
#pragma unroll
    for (int i = 0; i < 2; ++i) {
      const int u = w + 8 * i;
      if (u < 10) {
        const int ch = 10 * g + u;
        const v4u v = lds128(L, L_OB + lane * OS + 16 * u);
        stw(rs, v, (tok >> 4) * 20480 + (tok & 15) * 16, OFF_O + p * 81920 + (ch >> 2) * 1024 + (ch & 3) * 256);
      }
    }
  }
  drain();
  bar();
  if (tid == 0) publish(ws, OFF_F1 + (p * 8 + g) * 128, T);
  PHASE();

  const int s4 = w & 3, kh = w >> 2;
  const int tokg = 64 * p + 16 * s4 + t16;
  const int fo = kh == 0 ? 8 * g4 : 32 + 8 * g4;
  const v4u xa = *(const GAS v4u*)(x + (u32)(tokg * 1280 + (80 * g + fo) * 2));
  const v2u xb = *(const GAS v2u*)(x + (u32)(tokg * 1280 + (80 * g + 64 + 4 * g4) * 2));
  v4u pv[4]; v2u b2r = {0, 0};
  if (tid < 40) {
    const u32 vo = (tid >= 20 ? 3200 * 4 + (tid - 20) * 16 : 1280 * 4 + tid * 16) + g * 320;
#pragma unroll
    for (int k = 0; k < 4; ++k) pv[k] = ldw(rs, vo, OFF_MODP + k * 15360);
  }
  if (tid >= 64 && tid < 84) b2r = *(const GAS v2u*)(b2 + (80 * g + 4 * (tid - 64)) * 2);
  if (tid >= 128 && tid < 148) {
    const v2u cp = lds64(L, L_CPK + (80 * g + 4 * (tid - 128)) * 2);
    *(LAS f32x4*)(L + L_CF + (tid - 128) * 16) = f32x4{blo(cp.x), bhi(cp.x), blo(cp.y), bhi(cp.y)};
  }
  if (w == 6) {
    if (b < 108) {
      poll256(ws, OFF_F0B, lane, T);
      v4u uw[20];
#pragma unroll
      for (int j = 0; j < 20; ++j) { uw[j] = ldw(rs, l16 + (j & 1) * 2048, OFF_W1 + (b >> 1) * 40960 + (b & 1) * 1024 + (j >> 1) * 4096); SCHED(); }
      int ua = t16 < 2 ? (t16 == 0 ? L_CPK : L_SPK) + g4 * 16 : L_ONE + (t16 == 2 ? 0 : 16);
      const int ui = t16 < 2 ? 64 : 0;
      f32x4 ud = {0.f, 0.f, 0.f, 0.f};
#pragma unroll
      for (int j = 0; j < 20; ++j) { ud = mfma(uw[j], lds128(L, ua), ud); ua += ui; }
      if (t16 < 3) stw(rs, __builtin_bit_cast(v4u, ud), (((b >> 1) * 3 + t16) * 32 + 8 * g4 + 4 * (b & 1)) * 4, OFF_UVZ);
    }
  } else if (w == 7) {
    poll256(ws, OFF_F0B, lane, T);
    poll(ws, OFF_F1 + p * 8 * 128, 8, lane, T);
  }
  if (tid < 40) {
    f32x4 s = {0.f, 0.f, 0.f, 0.f};
#pragma unroll
    for (int k = 0; k < 4; ++k) s += __builtin_bit_cast(f32x4, pv[k]);
    *(LAS f32x4*)(L + L_SM + tid * 16) = s;
  }
  if (tid >= 64 && tid < 84) *(LAS f32x4*)(L + L_SM + 640 + (tid - 64) * 16) = f32x4{blo(b2r.x), bhi(b2r.x), blo(b2r.y), bhi(b2r.y)};
  bar();
  PHASE();
  v4u oa[10];
#pragma unroll
  for (int i = 0; i < 4; ++i) { oa[i] = ldw(rs, l16 + (i & 3) * 1024, OFF_O + (p * 4 + s4) * 20480 + kh * 10240 + (i >> 2) * 4096); SCHED(); }
  SCHED();
  f32x4 acc2[5];
#pragma unroll
  for (int i = 0; i < 5; ++i) acc2[i] = f32x4{0.f, 0.f, 0.f, 0.f};
  {
    v4u wf[2][5];
    const int wb2 = L_WO + kh * 51200 + l16;
#pragma unroll
    for (int ct = 0; ct < 5; ++ct) wf[0][ct] = lds128(L, wb2 + ct * 1024);
    SCHED();
#pragma unroll
    for (int i = 0; i < 10; ++i) {
#pragma unroll
      for (int ct = 0; ct < 5; ++ct) {
        mfma_acc(acc2[ct], wf[i & 1][ct], oa[i]);
        if (i + 1 < 10) wf[(i + 1) & 1][ct] = lds128(L, wb2 + ((i + 1) * 5 + ct) * 1024);
        if (ct == 1 && i + 4 < 10) oa[i + 4] = ldw(rs, l16 + ((i + 4) & 3) * 1024, OFF_O + (p * 4 + s4) * 20480 + kh * 10240 + ((i + 4) >> 2) * 4096);
        SCHED();
      }
    }
  }
  if (kh == 0) {
    *(LAS f32x4*)(L + L_RED + (s4 * 5 + 2) * 1024 + l16) = acc2[2];
    *(LAS f32x4*)(L + L_RED + (s4 * 5 + 3) * 1024 + l16) = acc2[3];
  } else {
    *(LAS f32x4*)(L + L_RED + (s4 * 5 + 0) * 1024 + l16) = acc2[0];
    *(LAS f32x4*)(L + L_RED + (s4 * 5 + 1) * 1024 + l16) = acc2[1];
    *(LAS f32x4*)(L + L_RED + (s4 * 5 + 4) * 1024 + l16) = acc2[4];
  }
  bar();
  PHASE();
  v4u x1a; v2u x1b = {0, 0};
  {
    const int c0 = kh == 0 ? 0 : 2;
    const f32x4 u0 = (kh == 0 ? acc2[0] : acc2[2]) + ldsf4(L, L_RED + (s4 * 5 + c0) * 1024 + l16);
    const f32x4 u1 = (kh == 0 ? acc2[1] : acc2[3]) + ldsf4(L, L_RED + (s4 * 5 + c0 + 1) * 1024 + l16);
    const f32x4 g0 = ldsf4(L, L_SM + fo * 4), g1 = ldsf4(L, L_SM + fo * 4 + 16);
    x1a.x = pk(fma2(g0.xy, u0.xy, unpk(xa.x)));
    x1a.y = pk(fma2(g0.zw, u0.zw, unpk(xa.y)));
    x1a.z = pk(fma2(g1.xy, u1.xy, unpk(xa.z)));
    x1a.w = pk(fma2(g1.zw, u1.zw, unpk(xa.w)));
    const u32 xco = OFF_XC + (p * 4 + s4) * 20480 + t16 * 16;
    const f32x4 cf0 = ldsf4(L, L_CF + fo * 4), cf1 = ldsf4(L, L_CF + fo * 4 + 16);
    const v4u cpk = lds128(L, L_CPK + (80 * g + fo) * 2), spk = lds128(L, L_SPK + (80 * g + fo) * 2);
    const v4u xc = {pk(unpk(x1a.x) * cf0.xy), pk(unpk(x1a.y) * cf0.zw), pk(unpk(x1a.z) * cf1.xy), pk(unpk(x1a.w) * cf1.zw)};
    { const int ch = 10 * g + (fo >> 3); stw(rs, xc, (ch >> 2) * 1024 + (ch & 3) * 256, xco); }
    const u32 one2 = 0x3f803f80u;
    float S1 = 0.f, S2 = 0.f, T1 = 0.f, T2 = 0.f, T3 = 0.f, T4 = 0.f;
#pragma unroll
    for (int e = 0; e < 4; ++e) {
      S1 = dot2(x1a[e], one2, S1); S2 = dot2(x1a[e], x1a[e], S2); T1 = dot2(xc[e], one2, T1);
      T2 = dot2(xc[e], xc[e], T2); T3 = dot2(xc[e], cpk[e], T3); T4 = dot2(xc[e], spk[e], T4);
    }
    if (kh == 0) {
      const f32x4 u2 = acc2[4] + ldsf4(L, L_RED + (s4 * 5 + 4) * 1024 + l16);
      const f32x4 g2 = ldsf4(L, L_SM + (64 + 4 * g4) * 4);
      x1b.x = pk(fma2(g2.xy, u2.xy, unpk(xb.x)));
      x1b.y = pk(fma2(g2.zw, u2.zw, unpk(xb.y)));
      const f32x4 cf2 = ldsf4(L, L_CF + (64 + 4 * g4) * 4);
      const v2u cp2 = lds64(L, L_CPK + (80 * g + 64 + 4 * g4) * 2), sp2 = lds64(L, L_SPK + (80 * g + 64 + 4 * g4) * 2);
      const v2u xc2 = {pk(unpk(x1b.x) * cf2.xy), pk(unpk(x1b.y) * cf2.zw)};
      const int ch = 10 * g + 8 + (g4 >> 1);
      __builtin_amdgcn_raw_buffer_store_b64(xc2, rs, xco + (ch >> 2) * 1024 + (ch & 3) * 256 + (g4 & 1) * 8, 0, SC1);
#pragma unroll
      for (int e = 0; e < 2; ++e) {
        S1 = dot2(x1b[e], one2, S1); S2 = dot2(x1b[e], x1b[e], S2); T1 = dot2(xc2[e], one2, T1);
        T2 = dot2(xc2[e], xc2[e], T2); T3 = dot2(xc2[e], cp2[e], T3); T4 = dot2(xc2[e], sp2[e], T4);
      }
    }
    const float m0 = x32_sum(swap16_add(S1, S2)), m1 = x32_sum(swap16_add(T1, T2)), m2 = x32_sum(swap16_add(T3, T4));
    if (g4 < 2) stw(rs, __builtin_bit_cast(v4u, f32x4{m0, m1, m2, 0.f}), ((2 * g + kh) * 2 + g4) * 1024 + (16 * s4 + t16) * 16, OFF_MOM + p * 32768);
  }
  drain();
  bar();
  if (tid == 0) publish(ws, OFF_F2 + (p * 8 + g) * 128, T);
  if (tid == 64 && b < 108) publish(ws, OFF_FU + b * 128, T);
  PHASE();

  const int j0 = g < 7 ? 7 * g : 48, nk = g < 6 ? 7 : 6;
  const bool act3 = w < nk;
  const int jb = j0 + (act3 ? w : 0);
  constexpr int R3 = 6;
  v4u w3[R3][2];
  const u32 w1o = OFF_W1 + jb * 40960;
  v4u b1v = {0, 0, 0, 0};
  if (act3) {
#pragma unroll
    for (int j = 0; j < R3; ++j) {
      w3[j][0] = ldw(rs, l16 + (j & 1) * 2048, w1o + (j >> 1) * 4096); w3[j][1] = ldw(rs, l16 + (j & 1) * 2048 + 1024, w1o + (j >> 1) * 4096);
      SCHED();
      if (j == 2) VMCNT(4);
    }
    b1v = *(const GAS v4u*)(b1 + (32 * jb + 8 * g4) * 2);
  }
  SCHED();
  if (w == 7) poll(ws, OFF_F2 + p * 8 * 128, 8, lane, T);
  bar();
  PHASE();
  {
    const u32 o1 = ((w >> 1) * 20 + 4 * (w & 1)) * 1024, o2 = ((w >> 1) * 20 + 8 + 6 * (w & 1)) * 1024;
    dma<4>(rs, l16, OFF_XC + p * 81920 + o1, L_A + o1);
    asm volatile("s_barrier" ::: "memory");
    dma<4>(rs, l16, OFF_XC + p * 81920 + o2, L_A + o2);
    dma<2>(rs, l16, OFF_XC + p * 81920 + o2 + 4096, L_A + o2 + 4096);
  }
  VMCNT(6);
  bar();
  PHASE();
  const u32 w2g = OFF_W2 + g * 54 * 5120;
  f32x4 acc3[4][2];
#pragma unroll
  for (int i = 0; i < 4; ++i) { acc3[i][0] = f32x4{0.f, 0.f, 0.f, 0.f}; acc3[i][1] = f32x4{0.f, 0.f, 0.f, 0.f}; }
  if (act3) {
    const int rdA = L_A + l16;
    auto rd = [&](int tt, int j) { return lds128(L, rdA + tt * 20480 + j * 1024); };
    v4u af[2][4];
#pragma unroll
    for (int tt = 0; tt < 4; ++tt) af[0][tt] = rd(tt, 0);
    SCHED();
#pragma unroll
    for (int j = 0; j < 20; ++j) {
      const int cu = j & 1, nx = cu ^ 1;
      const v4u wa = w3[j % R3][0], wb = w3[j % R3][1];
#pragma unroll
      for (int tt = 0; tt < 4; ++tt) {
        mfma_acc(acc3[tt][0], wa, af[cu][tt]);
        if (j + 1 < 20) af[nx][tt] = rd(tt, j + 1);
        SCHED();
        mfma_acc(acc3[tt][1], wb, af[cu][tt]);
        if (j + R3 < 20 && tt == 0) w3[j % R3][0] = ldw(rs, l16 + ((j + R3) & 1) * 2048, w1o + ((j + R3) >> 1) * 4096);
        if (j + R3 < 20 && tt == 2) w3[j % R3][1] = ldw(rs, l16 + ((j + R3) & 1) * 2048 + 1024, w1o + ((j + R3) >> 1) * 4096);
        SCHED();
      }
      if (j == 5) { VMCNT(12); bar(); }
    }
  } else if (w == 6) {
    VMCNT(0); bar();
  } else {
    v4u uv[3], mo[32];
#pragma unroll
    for (int i = 0; i < 32; ++i) { mo[i] = ldw(rs, l16 + (i & 3) * 1024, OFF_MOM + p * 32768 + (i >> 2) * 4096); SCHED(); }
    VMCNT(32); bar();
    poll(ws, OFF_FU + 2 * j0 * 128, 2 * nk, lane, T);
#pragma unroll
    for (int i = 0; i < 3; ++i) { uv[i] = ldw(rs, (i * 64 + lane < 24 * nk ? i * 64 + lane : 0) * 16, OFF_UVZ + j0 * 384); SCHED(); }
    float q5[5] = {0.f, 0.f, 0.f, 0.f, 0.f};
    {
      const u32 one2 = 0x3f803f80u;
      const int lo = (lane < 40 ? lane : 0) * 32;
#pragma unroll
      for (int hh = 0; hh < 2; ++hh) {
        const v4u cp = lds128(L, L_CPK + lo + hh * 16), sp = lds128(L, L_SPK + lo + hh * 16);
#pragma unroll
        for (int e = 0; e < 4; ++e) {
          q5[0] = dot2(cp[e], one2, q5[0]); q5[1] = dot2(cp[e], cp[e], q5[1]); q5[2] = dot2(cp[e], sp[e], q5[2]);
          q5[3] = dot2(sp[e], one2, q5[3]); q5[4] = dot2(sp[e], sp[e], q5[4]);
        }
      }
#pragma unroll
      for (int k = 0; k < 5; ++k) q5[k] = x32_sum(x16_sum(row16_sum(lane < 40 ? q5[k] : 0.f)));
    }
    f32x4 ma = {0.f, 0.f, 0.f, 0.f}, mb = {0.f, 0.f, 0.f, 0.f};
#pragma unroll
    for (int i = 0; i < 32; i += 2) { ma += __builtin_bit_cast(f32x4, mo[i]); mb += __builtin_bit_cast(f32x4, mo[i + 1]); }
    const float S1 = ma.x, T1 = ma.y, T3 = ma.z, S2 = mb.x, T2 = mb.y, T4 = mb.z;
    const float iD = 1.f / 640.f;
    const float mean = S1 * iD;
    const float r1 = __builtin_amdgcn_rsqf(fmaxf(S2 * iD - mean * mean, 0.f) + 1e-5f);
    const float H1 = r1 * (T1 - mean * q5[0]) + q5[3];
    const float H2 = r1 * r1 * (T2 - 2.f * mean * T3 + mean * mean * q5[1]) + 2.f * r1 * (T4 - mean * q5[2]) + q5[4];
    const float m2 = H1 * iD;
    const float r2 = __builtin_amdgcn_rsqf(fmaxf(H2 * iD - m2 * m2, 0.f) + 1e-5f);
    const float al = r1 * r2;
    *(LAS f32x4*)(L + L_ROWC + lane * 16) = f32x4{al, -mean * al, r2, -m2 * r2};
#pragma unroll
    for (int i = 0; i < 3; ++i) if (i * 64 + lane < 168) *(LAS v4u*)(L + L_UVZ + (i * 64 + lane) * 16) = uv[i];
  }
  bar();
  PHASE();
  if (act3) {
    const f32x2 bb[4] = {unpk(b1v.x), unpk(b1v.y), unpk(b1v.z), unpk(b1v.w)};
    const int uo = L_UVZ + w * 384 + g4 * 32;
    const f32x4 u0 = ldsf4(L, uo), u1 = ldsf4(L, uo + 16), v0 = ldsf4(L, uo + 128), v1 = ldsf4(L, uo + 144), z0 = ldsf4(L, uo + 256), z1 = ldsf4(L, uo + 272);
    const f32x2 uu[4] = {u0.xy, u0.zw, u1.xy, u1.zw}, vv[4] = {v0.xy, v0.zw, v1.xy, v1.zw}, zz[4] = {z0.xy, z0.zw, z1.xy, z1.zw};
    const float k1c = -2.f * 0.7978845608028654f * 1.4426950408889634f, k3c = k1c * 0.044715f;
#pragma unroll
    for (int tt = 0; tt < 4; ++tt) {
      const f32x4 rc = ldsf4(L, L_ROWC + (16 * tt + t16) * 16);
      const f32x2 al = {rc.x, rc.x}, be = {rc.y, rc.y}, r2 = {rc.z, rc.z}, ga2 = {rc.w, rc.w};
      v4u o;
#pragma unroll
      for (int e = 0; e < 4; ++e) {
        const f32x2 a2 = (e & 1) ? acc3[tt][e >> 1].zw : acc3[tt][e >> 1].xy;
        const f32x2 v = fma2(al, a2, fma2(be, uu[e], fma2(r2, vv[e], fma2(ga2, zz[e], bb[e]))));
        const f32x2 z = v * fma2(v * v, f32x2{k3c, k3c}, f32x2{k1c, k1c});
        const f32x2 d = f32x2{__builtin_amdgcn_exp2f(z.x), __builtin_amdgcn_exp2f(z.y)} + 1.f;
        o[e] = pk(v * f32x2{__builtin_amdgcn_rcpf(d.x), __builtin_amdgcn_rcpf(d.y)});
      }
      stw(rs, o, l16, OFF_HH + ((p * 4 + tt) * 54 + jb) * 1024);
    }
  } else if (w == 7) {
#pragma unroll 1
    for (int i = 0; i < 8; ++i) dma<4>(rs, l16, w2g + 4 * i * 1024, i * 4096);
  }
  if (w < 7) {
    const int f = 32 + 6 * w;
    dma<4>(rs, l16, w2g + f * 1024, f * 1024); dma<2>(rs, l16, w2g + (f + 4) * 1024, (f + 4) * 1024);
  } else {
    dma<1>(rs, l16, w2g + 74 * 1024, 74 * 1024);
    dma<4>(rs, l16, w2g + 27 * 5 * 1024, 76800); dma<1>(rs, l16, w2g + (27 * 5 + 4) * 1024, 76800 + 4096);
  }
  VMCNT(6);
  bar();
  if (tid == 0) publish(ws, OFF_F3 + (p * 8 + g) * 128, T);
  PHASE();

  const u32 w2o = w2g + 27 * kh * 5120;
  const u32 ring = kh * 76800;
  if (w < 7) {
#pragma unroll
    for (int i = 0; i < 3; ++i) {
      const int gi = w + 7 * i;
      if (gi < 17) dma<4>(rs, l16, w2g + (27 * 5 + 5 + 4 * gi) * 1024, 81920 + gi * 4096);
      else if (gi == 17) dma<2>(rs, l16, w2g + (27 * 5 + 5 + 68) * 1024, 81920 + 68 * 1024);
      if (i == 1) VMCNT(4);
    }
    VMCNT(0);
  } else {
    poll(ws, OFF_F3 + p * 8 * 128, 8, lane, T);
  }
  bar();
  PHASE();
  v4u ha[27];
  const u32 hho = OFF_HH + ((p * 4 + s4) * 54 + 27 * kh) * 1024;
  auto ld4 = [&](int i) { ha[i] = ldw(rs, l16 + (i & 3) * 1024, hho + (i >> 2) * 4096); };
#pragma unroll
  for (int i = 0; i < 6; ++i) {
    if (i == 2 || i == 4) asm volatile("s_barrier" ::: "memory");
    ld4(i); SCHED();
  }
  f32x4 acc4[5];
#pragma unroll
  for (int i = 0; i < 5; ++i) acc4[i] = f32x4{0.f, 0.f, 0.f, 0.f};
  {
    v4u wf[2][5];
    const int rb0 = ring + l16, rb1 = ring + 61440 + l16;
#pragma unroll
    for (int ct = 0; ct < 5; ++ct) wf[0][ct] = lds128(L, rb0 + ct * 1024);
    SCHED();
#pragma unroll
    for (int i = 0; i < 27; ++i) {
      const int n = i / 3;
#pragma unroll
      for (int ct = 0; ct < 5; ++ct) {
        mfma_acc(acc4[ct], wf[i & 1][ct], ha[i]);
        if (i + 1 < 27) wf[(i + 1) & 1][ct] = lds128(L, (((i + 1) % 15) < 12 ? rb0 : rb1) + (((i + 1) % 15) % 12 * 5 + ct) * 1024);
        if (i < 9 && ct == 1) ld4(6 + 2 * i);
        if (i < 9 && ct == 3) ld4(7 + 2 * i);
        if (i >= 9 && i < 12 && ct == 1) ld4(15 + i);
        SCHED();
      }
      if (i % 3 == 2) {
        if (n == 3) VMCNT(21);
        if (n == 4) VMCNT(15);
        if (n == 5) VMCNT(6);
        if (n == 6) VMCNT(0);
        bar();
        if (n < 4) {
          if (s4 < 3) dma<4>(rs, l16, w2o + (75 + 15 * n + 4 * s4) * 1024, ring + (15 * n + 4 * s4) * 1024);
          else dma<3>(rs, l16, w2o + (75 + 15 * n + 12) * 1024, ring + (15 * n + 12) * 1024);
        }
      }
    }
  }
  PHASE();
  if (kh == 0) {
    *(LAS f32x4*)(L + L_RED4 + (s4 * 5 + 2) * 1024 + l16) = acc4[2];
    *(LAS f32x4*)(L + L_RED4 + (s4 * 5 + 3) * 1024 + l16) = acc4[3];
  } else {
    *(LAS f32x4*)(L + L_RED4 + (s4 * 5 + 0) * 1024 + l16) = acc4[0];
    *(LAS f32x4*)(L + L_RED4 + (s4 * 5 + 1) * 1024 + l16) = acc4[1];
    *(LAS f32x4*)(L + L_RED4 + (s4 * 5 + 4) * 1024 + l16) = acc4[4];
  }
  bar();
  {
    const int c0 = kh == 0 ? 0 : 2;
    const f32x4 u0 = (kh == 0 ? acc4[0] : acc4[2]) + ldsf4(L, L_RED4 + (s4 * 5 + c0) * 1024 + l16);
    const f32x4 u1 = (kh == 0 ? acc4[1] : acc4[3]) + ldsf4(L, L_RED4 + (s4 * 5 + c0 + 1) * 1024 + l16);
    const f32x4 g0 = ldsf4(L, L_SM + 320 + fo * 4), g1 = ldsf4(L, L_SM + 320 + fo * 4 + 16);
    const f32x4 e0 = ldsf4(L, L_SM + 640 + fo * 4), e1 = ldsf4(L, L_SM + 640 + fo * 4 + 16);
    v4u o;
    o.x = pk(fma2(g0.xy, u0.xy + e0.xy, unpk(x1a.x)));
    o.y = pk(fma2(g0.zw, u0.zw + e0.zw, unpk(x1a.y)));
    o.z = pk(fma2(g1.xy, u1.xy + e1.xy, unpk(x1a.z)));
    o.w = pk(fma2(g1.zw, u1.zw + e1.zw, unpk(x1a.w)));
    *(GAS v4u*)(out + (u32)(tokg * 1280 + (80 * g + fo) * 2)) = o;
    if (kh == 0) {
      const f32x4 u2 = acc4[4] + ldsf4(L, L_RED4 + (s4 * 5 + 4) * 1024 + l16);
      const f32x4 g2 = ldsf4(L, L_SM + 320 + (64 + 4 * g4) * 4), e2 = ldsf4(L, L_SM + 640 + (64 + 4 * g4) * 4);
      const v2u o2 = {pk(fma2(g2.xy, u2.xy + e2.xy, unpk(x1b.x))), pk(fma2(g2.zw, u2.zw + e2.zw, unpk(x1b.y)))};
      *(GAS v2u*)(out + (u32)(tokg * 1280 + (80 * g + 64 + 4 * g4) * 2)) = o2;
    }
  }
  PHASE();
  asm volatile("fused_pf_end:" ::: "memory");
}

extern "C" void kernel_launch(void* const* d_in, const int* in_sizes, int n_in, void* d_out, int out_size, void* d_ws, size_t ws_size,
                              hipStream_t stream) {
  (void)in_sizes; (void)n_in; (void)out_size; (void)ws_size;
  (void)hipFuncSetAttribute((const void*)fused_block, hipFuncAttributeMaxDynamicSharedMemorySize, LDS_BYTES);
  fused_block<<<NWG, NTHR, LDS_BYTES, stream>>>((const u16*)d_in[0], (const u16*)d_in[1], (const u16*)d_in[2], (const u16*)d_in[4],
                                                (const u16*)d_in[5], (const u16*)d_in[6], (const u16*)d_in[7], (const u16*)d_in[8],
                                                (const u16*)d_in[9], (u16*)d_out, (unsigned char*)d_ws);
}
```
